```python
import torch

def _softmax(x, axis=-1):
    return torch.softmax(x, dim=axis)
def _gelu(x):
    return torch.nn.functional.gelu(x, approximate="tanh")
def _silu(x):
    return torch.nn.functional.silu(x)
def _sigmoid(x):
    return torch.sigmoid(x)
def _layernorm(x, axis=-1, eps=1e-5):
    m = x.mean(dim=axis, keepdim=True)
    v = x.var(dim=axis, keepdim=True, unbiased=False)
    return (x - m) / torch.sqrt(v + eps)
def _scatter_add(tgt, idx, src, axis=0):
    return tgt.clone().index_add_(axis, idx, src)
def _segment_sum(src, seg, n):
    out = torch.zeros((n,) + src.shape[1:], dtype=src.dtype)
    return out.index_add_(0, seg, src)
def _pad(x, pw):
    flat = tuple(v for pair in reversed(pw) for v in pair)
    return torch.nn.functional.pad(x, flat)

kernel_name = 'standalone__attention_interact_autoint__recsys'

def setup_inputs():

    _w = (lambda a: (lambda t: t.to(torch.bfloat16) if t.is_floating_point() else t)(torch.as_tensor(a)))
    B, F = 4096, 100
    D = max(2, (64 // 2) * 2)
    H = 2
    Dh = D // H
    return {
        "e": _w((lambda *s: torch.randn(*s, dtype=torch.bfloat16) * 0.05)(B, F, D).to(float)),
        "wq": _w((lambda *s: torch.randn(*s, dtype=torch.bfloat16) * 3.2)(H, D, Dh).to(float)),
        "wk": _w((lambda *s: torch.randn(*s, dtype=torch.bfloat16) * 3.2)(H, D, Dh).to(float)),
        "wv": _w((lambda *s: torch.randn(*s, dtype=torch.bfloat16) * 0.05)(H, D, Dh).to(float)),
        "wres": _w((lambda *s: torch.randn(*s, dtype=torch.bfloat16) * 0.05)(D, D).to(float)),
    }

def reference(e, wq, wk, wv, wres):
    H, D, Dh = wq.shape[0], wq.shape[1], wq.shape[2]
    B, F = e.shape[0], e.shape[1]
    q = torch.einsum('bfd,hdk->bhfk', e, wq)
    k = torch.einsum('bfd,hdk->bhfk', e, wk)
    v = torch.einsum('bfd,hdk->bhfk', e, wv)
    scale = float(Dh) ** -0.5
    scores = torch.einsum('bhfk,bhgk->bhfg', q, k) * scale
    attn = _softmax(scores, axis=-1)
    attn = (lambda x, dt: x.to(dt))(attn, v.dtype)
    ctx = torch.einsum('bhfg,bhgk->bhfk', attn, v)
    ctx = (lambda x, *d: x.permute(*d))(ctx, 0, 2, 1, 3).reshape(B, F, H * Dh)
    res = torch.einsum('bfd,de->bfe', e, wres)
    out = ctx + res
    mask = (lambda x, dt: x.to(dt))(out > 0, out.dtype)
    return out * mask
```

```cpp
#include <hip/hip_runtime.h>
#include <hip/hip_bf16.h>

typedef __attribute__((ext_vector_type(8))) __bf16 bf16x8;
typedef __attribute__((ext_vector_type(4))) __bf16 bf16x4;
typedef __attribute__((ext_vector_type(2))) __bf16 bf16x2;
typedef __attribute__((ext_vector_type(4))) short s16x4;
typedef __attribute__((ext_vector_type(2))) short s16x2;
typedef __attribute__((ext_vector_type(16))) float f32x16;
typedef __attribute__((ext_vector_type(4))) float f32x4;
typedef __attribute__((ext_vector_type(2))) float f32x2;
typedef __attribute__((ext_vector_type(4))) unsigned u32x4;
typedef __attribute__((ext_vector_type(2))) unsigned u32x2;

#define DI __device__ __forceinline__
#define PIN(x) asm volatile("" : "+v"(x))
#define MFMA32(a, b, c) __builtin_amdgcn_mfma_f32_32x32x16_bf16((a), (b), (c), 0, 0, 0)
#define MFMA16(a, b, c) __builtin_amdgcn_mfma_f32_16x16x32_bf16((a), (b), (c), 0, 0, 0)
#define MFMA32K8(a, b, c) __builtin_amdgcn_mfma_f32_32x32x8bf16_1k((a), (b), (c), 0, 0, 0)
#define MFMA16K16(a, b, c) __builtin_amdgcn_mfma_f32_16x16x16bf16_1k((a), (b), (c), 0, 0, 0)

constexpr int F = 100, D = 64;
constexpr int ROWB = 128;
constexpr int SAMPLEB = F * ROWB;
constexpr int NWAVE = 8;
constexpr int LDS_W = 0;
constexpr int LDS_Z = 32768;
constexpr int LDS_E = 33024;
constexpr int EWAVE = 13312;
constexpr int LDS_BYTES = LDS_E + NWAVE * EWAVE;
constexpr float SCL = 0.17677669529663687f * 1.4426950408889634f;

typedef __attribute__((address_space(3))) const bf16x8* lds_frag_ptr;
typedef __attribute__((address_space(3))) char* lds_ptr;

DI unsigned pk2(float lo, float hi) {
  f32x2 v = {lo, hi};
  return __builtin_bit_cast(unsigned, __builtin_convertvector(v, bf16x2));
}
DI bf16x8 pack8(const f32x16& x, int s) {
  u32x4 p = {pk2(x[8 * s], x[8 * s + 1]), pk2(x[8 * s + 2], x[8 * s + 3]), pk2(x[8 * s + 4], x[8 * s + 5]), pk2(x[8 * s + 6], x[8 * s + 7])};
  return __builtin_bit_cast(bf16x8, p);
}
DI bf16x8 ldsfrag(unsigned addr) { return *(lds_frag_ptr)(size_t)addr; }
DI float max3(float a, float b, float c) { return __builtin_fmaxf(__builtin_fmaxf(a, b), c); }

template <int OFF> DI void glds16(const char* sbase, unsigned voff, unsigned ldsbase) {
  unsigned keep;
  asm volatile("s_mov_b32 %0, m0\n\ts_mov_b32 m0, %3\n\ts_nop 0\n\tglobal_load_lds_dwordx4 %1, %2 offset:%4\n\ts_mov_b32 m0, %0"
               : "=&s"(keep) : "v"(voff), "s"(sbase), "s"(ldsbase), "i"(OFF) : "memory");
}
DI void load_tile(const char* src, unsigned voff_e, unsigned voff_o, unsigned ldsbase) {
  glds16<0>(src, voff_e, ldsbase);
  glds16<1024>(src, voff_o, ldsbase);
  glds16<2048>(src, voff_e, ldsbase);
  glds16<3072>(src, voff_o, ldsbase);
}

struct KV {
  bf16x8 k[2][3][2];
  bf16x8 kl[2][2];
  bf16x8 v[2][3][2];
  unsigned vl[2];
};

DI void softmax_pack(f32x16 (&s)[3], f32x4& sl, bf16x8 (&p)[3][2], unsigned& pl, float& lsum) {
  float m0 = max3(s[0][0], s[0][1], sl[0]), m1 = max3(s[0][2], s[0][3], sl[1]);
#pragma unroll
  for (int i = 4; i < 16; i += 4) { m0 = max3(m0, s[0][i], s[0][i + 1]); m1 = max3(m1, s[0][i + 2], s[0][i + 3]); }
#pragma unroll
  for (int t = 1; t < 3; ++t)
#pragma unroll
    for (int i = 0; i < 16; i += 4) { m0 = max3(m0, s[t][i], s[t][i + 1]); m1 = max3(m1, s[t][i + 2], s[t][i + 3]); }
  float m = __builtin_fmaxf(m0, m1);
  {
    auto r = __builtin_amdgcn_permlane32_swap(__float_as_uint(m), __float_as_uint(m), false, false);
    m = __builtin_fmaxf(__uint_as_float(r[0]), __uint_as_float(r[1]));
  }
  const float nm = -m * SCL;
  float a0 = 0.f, a1 = 0.f;
#pragma unroll
  for (int t = 0; t < 3; ++t) {
#pragma unroll
    for (int i = 0; i < 16; ++i) s[t][i] = __builtin_amdgcn_exp2f(__builtin_fmaf(s[t][i], SCL, nm));
#pragma unroll
    for (int i = 0; i < 16; i += 2) { a0 += s[t][i]; a1 += s[t][i + 1]; }
    p[t][0] = pack8(s[t], 0); p[t][1] = pack8(s[t], 1);
  }
  const float e0 = __builtin_amdgcn_exp2f(__builtin_fmaf(sl[0], SCL, nm)), e1 = __builtin_amdgcn_exp2f(__builtin_fmaf(sl[1], SCL, nm));
  a0 += e0; a1 += e1;
  pl = pk2(e0, e1);
  float l = a0 + a1;
  {
    auto r = __builtin_amdgcn_permlane32_swap(__float_as_uint(l), __float_as_uint(l), false, false);
    l = __uint_as_float(r[0]) + __uint_as_float(r[1]);
  }
  lsum = l;
}

DI f32x16 pv(const KV& kv, int h, const bf16x8 (&p)[3][2], unsigned pl) {
  f32x16 o = {};
#pragma unroll
  for (int t = 0; t < 3; ++t) { o = MFMA32(kv.v[h][t][0], p[t][0], o); o = MFMA32(kv.v[h][t][1], p[t][1], o); }
  u32x2 a = {kv.vl[h], 0u}, b = {pl, 0u};
  o = MFMA32K8(__builtin_bit_cast(s16x4, a), __builtin_bit_cast(s16x4, b), o);
  return o;
}

DI unsigned relu2(unsigned x) {
  s16x2 z = {0, 0};
  return __builtin_bit_cast(unsigned, __builtin_elementwise_max(__builtin_bit_cast(s16x2, x), z));
}
DI void finish(const f32x16& o, const f32x16& r, float inv, u32x4& w0, u32x4& w1) {
  float y[16];
#pragma unroll
  for (int i = 0; i < 16; ++i) y[i] = __builtin_fmaf(o[i], inv, r[i]);
#pragma unroll
  for (int i = 0; i < 4; ++i) { w0[i] = relu2(pk2(y[2 * i], y[2 * i + 1])); w1[i] = relu2(pk2(y[8 + 2 * i], y[8 + 2 * i + 1])); }
}

__global__ __launch_bounds__(512, 2) void autoint_kernel(const char* __restrict__ e, const unsigned short* __restrict__ wq,
                                                         const unsigned short* __restrict__ wk, const unsigned short* __restrict__ wv,
                                                         const unsigned short* __restrict__ wres, char* __restrict__ out, int B) {
  __shared__ __attribute__((aligned(256))) char lds[LDS_BYTES];
  const unsigned lds0 = (unsigned)(size_t)(lds_ptr)lds;
  const int tid = threadIdx.x, lane = tid & 63;
  const int wave = __builtin_amdgcn_readfirstlane(tid >> 6);
  const int r = lane & 31, hf = lane >> 5;
  const int nw = gridDim.x * NWAVE;
  int b = blockIdx.x * NWAVE + wave;
  const unsigned ebase = lds0 + LDS_E + wave * EWAVE;

  const unsigned voff_e = (lane >> 3) * 128 + (((lane & 7) ^ (lane >> 4)) << 4), voff_o = voff_e ^ 64;
  const unsigned voff_l = lane * 16;
  if (b < B) {
    const char* src = e + (size_t)b * SAMPLEB;
    load_tile(src, voff_e, voff_o, ebase);
    load_tile(src + 4096, voff_e, voff_o, ebase + 4096);
    load_tile(src + 8192, voff_e, voff_o, ebase + 8192);
    if (lane < 32) glds16<0>(src + 12288, voff_l, ebase + 12288);
  }
  {
    const int mat = wave >> 1, head = wave & 1;
    const int rs = (r & 0x13) | ((r & 4) << 1) | ((r & 8) >> 1);
    const unsigned short* src; int ld;
    if (mat == 0) { src = wq + head * 2048 + r; ld = 32; }
    else if (mat == 1) { src = wk + head * 2048 + r; ld = 32; }
    else if (mat == 2) { src = wv + head * 2048 + rs; ld = 32; }
    else { src = wres + head * 32 + rs; ld = 64; }
#pragma unroll
    for (int s = 0; s < 4; ++s) {
      u32x4 w;
#pragma unroll
      for (int j = 0; j < 4; ++j) {
        const int d = 16 * s + 8 * hf + 2 * j;
        w[j] = (unsigned)src[d * ld] | ((unsigned)src[(d + 1) * ld] << 16);
      }
      *(u32x4*)(lds + LDS_W + (wave * 4 + s) * 1024 + lane * 16) = w;
    }
    if (tid < 16) *(u32x4*)(lds + LDS_Z + tid * 16) = u32x4{0, 0, 0, 0};
  }
  asm volatile("s_waitcnt vmcnt(0)" ::: "memory");
  __syncthreads();

  const unsigned wadr = lds0 + LDS_W + lane * 16;
  unsigned eadr[4];
#pragma unroll
  for (int s = 0; s < 4; ++s) eadr[s] = ebase + r * 128 + (((2 * s + hf) ^ ((r >> 1) & 7)) << 4);
  const bool lval = ((r & 2) == 0) && (((r >> 2) & 1) == (r >> 4));
  const int ltok = (r & 1) + 2 * ((r >> 3) & 1);
  const unsigned zadr = lds0 + LDS_Z + hf * 16;
  const unsigned lrel = ebase + 12288 + ltok * 128 + hf * 16;
  const bool vval = (r & 0x1a) == 0;
  const unsigned vrel = ebase + 12288 + ((r & 1) + 2 * ((r >> 2) & 1)) * 128 + hf * 16;
  const unsigned ooff = r * 128 + hf * 16;
  const unsigned ooff_l = 12288 + ltok * 128 + (r >> 4) * 64 + hf * 16;
#define WF(mat, h, s) ldsfrag(wadr + (((mat) * 2 + (h)) * 4 + (s)) * 1024)

  int par = 0;
  for (; b < B; b += nw) {
    const int bn = b + nw;
    const bool more = bn < B;
    const char* nsrc = e + (size_t)bn * SAMPLEB;
    char* dst = out + (size_t)b * SAMPLEB;
    const unsigned lk = lval ? lrel + par * 512 : zadr;
    const unsigned l0 = (lval && r < 16) ? lrel + par * 512 : zadr;
    const unsigned l1 = (lval && r >= 16) ? lrel + par * 512 : zadr;
    const unsigned lv = vval ? vrel + par * 512 : zadr;
    asm volatile("s_waitcnt vmcnt(6)" ::: "memory");

    KV kv;
    {
      bf16x8 ef[3][4];
#pragma unroll
      for (int t = 0; t < 3; ++t)
#pragma unroll
        for (int s = 0; s < 4; ++s) ef[t][s] = ldsfrag(eadr[s] + t * 4096);
#pragma unroll
      for (int h = 0; h < 2; ++h) {
        bf16x8 w[4];
#pragma unroll
        for (int s = 0; s < 4; ++s) w[s] = WF(1, h, s);
#pragma unroll
        for (int t = 0; t < 3; ++t) {
          f32x16 a = {};
#pragma unroll
          for (int s = 0; s < 4; ++s) a = MFMA32(w[s], ef[t][s], a);
          kv.k[h][t][0] = pack8(a, 0); kv.k[h][t][1] = pack8(a, 1); PIN(kv.k[h][t][0]); PIN(kv.k[h][t][1]);
        }
        {
          f32x16 a = {};
#pragma unroll
          for (int s = 0; s < 4; ++s) a = MFMA32(w[s], ldsfrag(lk + s * 32), a);
          kv.kl[h][0] = pack8(a, 0); kv.kl[h][1] = pack8(a, 1); PIN(kv.kl[h][0]); PIN(kv.kl[h][1]);
        }
#pragma unroll
        for (int s = 0; s < 4; ++s) w[s] = WF(2, h, s);
#pragma unroll
        for (int t = 0; t < 3; ++t) {
          f32x16 a = {};
#pragma unroll
          for (int s = 0; s < 4; ++s) a = MFMA32(ef[t][s], w[s], a);
          kv.v[h][t][0] = pack8(a, 0); kv.v[h][t][1] = pack8(a, 1); PIN(kv.v[h][t][0]); PIN(kv.v[h][t][1]);
        }
        {
          f32x16 a = {};
#pragma unroll
          for (int s = 0; s < 4; ++s) a = MFMA32(ldsfrag(lv + s * 32), w[s], a);
          kv.vl[h] = pk2(a[0], a[1]); PIN(kv.vl[h]);
        }
      }
    }
#pragma unroll
    for (int t = 0; t < 3; ++t) {
      bf16x8 ef[4];
#pragma unroll
      for (int s = 0; s < 4; ++s) ef[s] = ldsfrag(eadr[s] + t * 4096);
      asm volatile("s_waitcnt lgkmcnt(0)" : "+v"(ef[0]), "+v"(ef[1]), "+v"(ef[2]), "+v"(ef[3]) :: "memory");
      if (more) {
        load_tile(nsrc + t * 4096, voff_e, voff_o, ebase + t * 4096);
        if (t == 0 && lane < 32) glds16<0>(nsrc + 12288, voff_l, ebase + 12288 + (par ^ 1) * 512);
      }
#pragma unroll
      for (int h = 0; h < 2; ++h) {
        f32x16 a = {};
#pragma unroll
        for (int s = 0; s < 4; ++s) a = MFMA32(WF(0, h, s), ef[s], a);
        const bf16x8 q0 = pack8(a, 0), q1 = pack8(a, 1);
        f32x16 sc[3];
#pragma unroll
        for (int u = 0; u < 3; ++u) { f32x16 z = {}; sc[u] = MFMA32(kv.k[h][u][0], q0, z); sc[u] = MFMA32(kv.k[h][u][1], q1, sc[u]); }
        f32x4 sl = {};
        sl = MFMA16(kv.kl[h][0], q0, sl); sl = MFMA16(kv.kl[h][1], q1, sl);
        f32x16 res = {};
#pragma unroll
        for (int s = 0; s < 4; ++s) res = MFMA32(WF(3, h, s), ef[s], res);
        bf16x8 p[3][2]; unsigned pl; float l;
        softmax_pack(sc, sl, p, pl, l);
        const f32x16 o = pv(kv, h, p, pl);
        u32x4 w0, w1;
        finish(o, res, __builtin_amdgcn_rcpf(l), w0, w1);
        char* d = dst + t * 4096 + h * 64 + ooff;
        *(u32x4*)d = w0; *(u32x4*)(d + 32) = w1;
      }
    }
    {
      bf16x8 q[2][2]; f32x16 res = {};
#pragma unroll
      for (int h = 0; h < 2; ++h) {
        f32x16 a = {};
#pragma unroll
        for (int s = 0; s < 4; ++s) {
          const bf16x8 ef = ldsfrag((h ? l1 : l0) + s * 32);
          a = MFMA32(WF(0, h, s), ef, a);
          res = MFMA32(WF(3, h, s), ef, res);
        }
        q[h][0] = pack8(a, 0); q[h][1] = pack8(a, 1);
      }
      f32x16 sc[3];
#pragma unroll
      for (int u = 0; u < 3; ++u) {
        f32x16 z = {};
        sc[u] = MFMA32(kv.k[0][u][0], q[0][0], z); sc[u] = MFMA32(kv.k[0][u][1], q[0][1], sc[u]);
        sc[u] = MFMA32(kv.k[1][u][0], q[1][0], sc[u]); sc[u] = MFMA32(kv.k[1][u][1], q[1][1], sc[u]);
      }
      f32x4 sl = {};
      sl = MFMA16(kv.kl[0][0], q[0][0], sl); sl = MFMA16(kv.kl[0][1], q[0][1], sl);
      sl = MFMA16(kv.kl[1][0], q[1][0], sl); sl = MFMA16(kv.kl[1][1], q[1][1], sl);
      bf16x8 p[3][2]; unsigned pl; float l;
      softmax_pack(sc, sl, p, pl, l);
      const f32x16 o0 = pv(kv, 0, p, pl), o1 = pv(kv, 1, p, pl);
      f32x16 o;
#pragma unroll
      for (int i = 0; i < 16; ++i) o[i] = r < 16 ? o0[i] : o1[i];
      u32x4 w0, w1;
      finish(o, res, __builtin_amdgcn_rcpf(l), w0, w1);
      if (lval) { char* d = dst + ooff_l; *(u32x4*)d = w0; *(u32x4*)(d + 32) = w1; }
    }
    par ^= 1;
  }
}

extern "C" void kernel_launch(void* const* d_in, const int* in_sizes, int n_in, void* d_out, int out_size, void* d_ws, size_t ws_size,
                              hipStream_t stream) {
  const int B = in_sizes[0] / (F * D);
  int grid = (B + NWAVE - 1) / NWAVE;
  if (grid > 256) grid = 256;
  autoint_kernel<<<grid, 512, 0, stream>>>((const char*)d_in[0], (const unsigned short*)d_in[1], (const unsigned short*)d_in[2],
                                           (const unsigned short*)d_in[3], (const unsigned short*)d_in[4], (char*)d_out, B);
}
```

```cpp
#include <hip/hip_runtime.h>
#include <hip/hip_bf16.h>

typedef __attribute__((ext_vector_type(4))) unsigned u32x4;
typedef __attribute__((ext_vector_type(16))) unsigned u32x16;
typedef __attribute__((address_space(3))) char* lds_ptr;
#define DI __device__ __forceinline__

constexpr int F = 100, D = 64;
constexpr int SAMPLEB = F * 128;
constexpr int NWAVE = 4;
constexpr int LDS_W = 0;
constexpr int LDS_Z = 32768;
constexpr int LEFTB = 9216;
constexpr int ZB = LEFTB + 256;
constexpr int LDS_E = LDS_Z + ZB;
constexpr int EWAVE = 12288 + LEFTB + 8192;
constexpr int LDS_BYTES = LDS_E + NWAVE * EWAVE;
#define CHK_LIMIT 0x1p40f

typedef __attribute__((ext_vector_type(2))) __bf16 bf16x2_t;
typedef __attribute__((ext_vector_type(2))) float f32x2_t;
DI unsigned scale2(unsigned w) {
  const float c = 0.17677669529663687f * 1.4426950408889634f;
  f32x2_t v = {__uint_as_float(w << 16) * c, __uint_as_float(w & 0xffff0000u) * c};
  return __builtin_bit_cast(unsigned, __builtin_convertvector(v, bf16x2_t));
}
#define TILE_LOADS(P) "global_load_lds_dwordx4 %2, " P " nt\n\tglobal_load_lds_dwordx4 %3, " P " offset:1024 nt\n\t" \
                      "global_load_lds_dwordx4 %2, " P " offset:2048 nt\n\tglobal_load_lds_dwordx4 %3, " P " offset:3072 nt\n\t"
template <int EXHI> DI void load_first(const char* src, unsigned wadr, unsigned voff_e, unsigned voff_o, unsigned ebase) {
  asm volatile("s_add_u32 m0, %4, 12288\n\ts_mov_b32 exec_hi, %6\n\tglobal_load_lds_dwordx4 %5, %1 nt\n\ts_mov_b32 exec_hi, -1\n\t"
               "s_mov_b32 m0, %4\n\ts_nop 0\n\t" TILE_LOADS("%0")
               :: "s"(src), "s"(src + 12288), "v"(voff_e), "v"(voff_o), "s"(ebase), "v"(wadr), "i"(EXHI) : "memory", "scc");
}
DI void load_rest(const char* src, unsigned voff_e, unsigned voff_o, unsigned ebase) {
  asm volatile("s_add_u32 m0, %4, 4096\n\ts_nop 0\n\t" TILE_LOADS("%0")
               "s_add_u32 m0, %4, 8192\n\ts_nop 0\n\t" TILE_LOADS("%1")
               :: "s"(src + 4096), "s"(src + 8192), "v"(voff_e), "v"(voff_o), "s"(ebase) : "memory", "scc");
}

DI void load_tails(const char* src, unsigned l0, unsigned l1, unsigned ebase) {
  asm volatile("s_add_u32 m0, %3, %4\n\ts_nop 0\n\tglobal_load_lds_dwordx4 %1, %0 nt\n\ts_add_u32 m0, %3, %5\n\ts_nop 0\n\tglobal_load_lds_dwordx4 %2, %0 nt"
               :: "s"(src), "v"(l0), "v"(l1), "s"(ebase), "i"(12288 + LEFTB), "i"(12288 + LEFTB + 1024) : "memory", "scc");
}

__global__ __launch_bounds__(256, 1) void autoint_kernel(const char* e_, const char* wq_, const char* wk_, const char* wv_,
                                                         const char* wres_, char* out_, int nsamples_, int nwaves_, int last_, int unused_) {
  __shared__ __attribute__((aligned(1024))) char lds[LDS_BYTES];
  u32x16 ka; u32x4 q0, q1;
  unsigned voff_e, voff_o, ebase, wave64, b; const char* src0;
  { unsigned t0, t1; unsigned long long pc;
    asm volatile(
      "s_load_dwordx16 %0, %12, 0x0\n\tv_readfirstlane_b32 %6, %13\n\ts_getpc_b64 %11\n\tv_lshrrev_b32 %9, 5, %13\n\t"
      "s_and_b32 %7, %14, 0xf8\n\tv_add_u32 %9, %7, %9\n\tv_mul_u32_u24 %9, 88, %9\n\tglobal_load_dword a255, %9, %11\n\t"
      "v_lshlrev_b32 %10, 4, %13\n\ts_lshl_b32 %7, %6, 4\n\tv_add_u32 %9, %7, %10\n\ts_lshl_b32 %7, %6, 5\n\ts_add_u32 m0, %7, 0x2000\n\t"
      "s_waitcnt lgkmcnt(0)\n\t"
      "global_load_dwordx4 %1, %9, s[10:11]\n\tglobal_load_dwordx4 %2, %9, s[10:11] offset:1024\n\t"
      "global_load_lds_dwordx4 %9, s[12:13]\n\tglobal_load_lds_dwordx4 %9, s[12:13] offset:1024\n\t"
      "s_addk_i32 m0, 0x2000\n\ts_lshl_b32 %7, %14, 2\n\t"
      "global_load_lds_dwordx4 %9, s[14:15]\n\tglobal_load_lds_dwordx4 %9, s[14:15] offset:1024\n\t"
      "s_addk_i32 m0, 0x2000\n\ts_lshr_b32 %5, %6, 6\n\t"
      "global_load_lds_dwordx4 %9, s[16:17]\n\tglobal_load_lds_dwordx4 %9, s[16:17] offset:1024\n\t"
      "s_add_i32 %7, %7, %5\n\ts_mulk_i32 %5, %15\n\ts_add_u32 %5, %5, %16\n\ts_mov_b32 m0, %5\n\t"
      "s_min_i32 s25, %7, s22\n\ts_mul_i32 s24, s25, 12800\n\ts_mul_hi_u32 s25, s25, 12800\n\ts_add_u32 s24, s24, s8\n\ts_addc_u32 s25, s25, s9\n\t"
      "v_and_b32 %9, 0x30, %13\n\tv_xor_b32 %10, %9, %10\n\tv_and_b32 %3, 0x3f0, %10\n\tv_xor_b32 %4, 64, %3\n\t"
      "global_load_lds_dwordx4 %3, %8 nt\n\tglobal_load_lds_dwordx4 %4, %8 offset:1024 nt\n\t"
      "global_load_lds_dwordx4 %3, %8 offset:2048 nt\n\tglobal_load_lds_dwordx4 %4, %8 offset:3072 nt"
      : "=&{s[8:23]}"(ka), "=&v"(q0), "=&v"(q1), "=&v"(voff_e), "=&v"(voff_o), "=&s"(ebase), "=&s"(wave64), "=&s"(b), "=&{s[24:25]}"(src0),
        "=&v"(t0), "=&v"(t1), "=&s"(pc)
      : "s"(__builtin_amdgcn_kernarg_segment_ptr()), "v"(threadIdx.x), "s"(blockIdx.x), "i"(EWAVE), "i"(LDS_E)
      : "memory", "scc", "a255");
  }
#define KPTR(i) ((char*)(((unsigned long long)ka[2 * (i) + 1] << 32) | ka[2 * (i)]))
  const unsigned lds0 = (unsigned)(size_t)(lds_ptr)lds;
  const int tid = threadIdx.x, lane = tid & 63, wave = wave64 >> 6;
  int nw = (int)ka[13];
  const int bend = (int)ka[12];
  unsigned step = (unsigned)nw * SAMPLEB;
  const int more = ((int)b + nw < bend) + ((int)b + 2 * nw < bend) + ((int)b + 3 * nw < bend);
  const unsigned loff = 12288 + (lane & 31) * 16;
  load_tails(src0, loff + min(lane >> 5, more) * step, loff + min(2 + (lane >> 5), more) * step, ebase);
  char* const dst0 = KPTR(5) + (size_t)b * SAMPLEB;

  int lm = lane; asm volatile("" : "+v"(lm));
  const int r_ = lm & 31, hf_ = lm >> 5, G_ = lm >> 4, c_ = lm & 15;
  const unsigned wadr = lm * 16;
  unsigned ea0 = ebase + r_ * 128 + ((hf_ ^ ((r_ >> 1) & 7)) << 4), ea1 = ea0 ^ 32, ea2 = ea0 ^ 64, ea3 = ea0 ^ 96;
  const unsigned lbase = ebase + 12288, zbase = lds0 + LDS_Z + 64;
  const int chead = c_ >> 3, ctok = (c_ >> 1) & 3, csig = c_ & 1;
  const bool cval = (G_ & 1) == csig;
  unsigned lh0 = ((cval && chead == 0) ? lbase + ctok * 144 : zbase) + hf_ * 16;
  unsigned lh1 = ((cval && chead == 1) ? lbase + ctok * 144 : zbase) + hf_ * 16;
  const bool kval = ((c_ & 2) == 0) && (((c_ >> 2) & 1) == (G_ & 1));
  unsigned lk = (kval ? lbase + (2 * (c_ >> 3) + (c_ & 1)) * 144 : zbase) + hf_ * 16;
  const unsigned lin = chead * 64 + csig * 32 + (G_ & 1) * 16 + (G_ >> 1) * 8;
  unsigned lw = lbase + ctok * 144 + lin;
  unsigned pk = ctok * 128 + lin;
  unsigned sadr = ebase + lm * 16;
  unsigned radr = ebase + (lm & 3) * 512 + ((lm & 4) ? 0 : 2048) + (lm >> 3) * 16;
  unsigned vadr = lbase + hf_ * 256 + r_ * 8, pw = lbase + ((r_ >> 2) & 3) * 2304 + (r_ & 3) * 144 + (r_ >> 4) * 64 + hf_ * 16, pv = lbase + hf_ * 2304 + r_ * 8;
  const unsigned pa = lbase + LEFTB + (r_ & 15) * 128;
  unsigned pe = (r_ < 16 ? pa : zbase) + hf_ * 16, pe1 = (r_ < 16 ? zbase : pa) + hf_ * 16;
  unsigned sel1 = ((((lm >> 4) & 1) == ((lm >> 2) & 1)) && !(lm & 1)) ? 0x3f803f80u : 0u;
  unsigned sela = (lm & 4) ? 0u : 0x3f803f80u, selb = (lm & 4) ? 0x3f803f80u : 0u, sidx = (lm & 1) ? 0xeeeeeeeeu : 0x44444444u;
  const int tq = (lm >> 2) & 3, tp = lm & 3, tps = ((tp & 1) << 1) | (tp >> 1);
  unsigned ta0 = lds0 + LDS_W + (8 * hf_ + tq) * 64 + (G_ & 1) * 32 + tp * 8, ta1 = lds0 + LDS_W + (8 * hf_ + tq) * 64 + (G_ & 1) * 32 + tps * 8;
  unsigned ta2 = ta1 + (8 * hf_ + tq) * 64;
  *(u32x4*)(lds + LDS_Z + tid * 16) = u32x4{0, 0, 0, 0}; *(u32x4*)(lds + LDS_Z + 4096 + tid * 16) = u32x4{0, 0, 0, 0};
  *(u32x4*)(lds + LDS_Z + ZB - 4096 + tid * 16) = u32x4{0, 0, 0, 0};
  asm volatile("" : "+v"(ea0), "+v"(ea1), "+v"(ea2), "+v"(ea3), "+v"(ta2), "+v"(lh0), "+v"(lh1), "+v"(lk), "+v"(lw), "+v"(pk), "+v"(sadr), "+v"(radr), "+v"(vadr), "+v"(pw), "+v"(pv), "+v"(pe), "+v"(pe1),
               "+v"(sel1), "+v"(sela), "+v"(selb), "+v"(sidx), "+v"(ta0), "+v"(ta1) :: "memory");

  asm volatile("s_waitcnt vmcnt(12)" : "+v"(q0), "+v"(q1) :: "memory");
#pragma unroll
  for (int j = 0; j < 4; ++j) { q0[j] = scale2(q0[j]); q1[j] = scale2(q1[j]); }
  *(u32x4*)(lds + LDS_W + wave * 2048 + wadr) = q0; *(u32x4*)(lds + LDS_W + wave * 2048 + 1024 + wadr) = q1;
  asm volatile("s_waitcnt vmcnt(6)" ::: "memory");
  asm volatile("s_waitcnt lgkmcnt(0)\n\ts_barrier" ::: "memory");
  if ((int)b >= bend) return;

  const char* nsrc = src0 + step;
  char* dst = dst0;
  int bb = b;
  float chk;
  unsigned alt = lds0 + LDS_W + wave * 8192;
  asm volatile(
    "s_mov_b32 s54, 0xaaaa5555\n"
    "s_mov_b32 s55, 0xaaaa5555\n"
    "s_mov_b32 s56, 0x00ff00ff\n"
    "s_mov_b32 s57, 0x00ff00ff\n"
    "s_mov_b32 s61, 0xffff0000\n"
    "s_mov_b32 s62, -1\n"
    "s_mov_b32 s63, 15\n"
    "v_mov_b32 v227, 0\n"
    "v_mov_b32 v231, 0\n"
    "v_mov_b32 v233, 0\n"
    "s_add_u32 s53, s52, 12288\n"
    "s_movk_i32 s96, 4096\n"
    "s_movk_i32 s97, 8192\n"
    "s_movk_i32 s98, 12288\n"
    "s_add_u32 s99, s39, 12288\n"
    "s_movk_i32 s60, 12800\n"
    "s_movk_i32 s58, 2304\n"
    "s_mov_b32 s47, 0x20000\n"
    "s_mov_b32 s91, 0x20000\n"
    "s_mov_b32 s95, 0x20000\n"
    "s_mov_b64 s[44:45], s[42:43]\n"
    "s_add_u32 s46, s39, 12800\n"
    "s_add_i32 s59, s37, s38\n"
    "s_add_i32 s37, s59, s38\n"
    "s_mov_b64 s[88:89], s[40:41]\n"
    "s_cmp_lt_i32 s59, s36\n"
    "s_cselect_b32 s90, s60, 0\n"
    "s_sub_u32 s92, s40, s39\n"
    "s_subb_u32 s93, s41, 0\n"
    "s_mov_b32 s94, s60\n"
    "s_lshl_b32 s34, s39, 1\n"
    "s_add_u32 s48, s40, s34\n"
    "s_addc_u32 s49, s41, 0\n"
    "s_add_i32 s34, s37, s38\n"
    "s_cmp_lt_i32 s34, s36\n"
    "s_cselect_b32 s50, s60, 0\n"
    "s_mov_b32 s51, 0x20000\n"
    "s_sub_u32 s86, s35, s52\n"
    "s_mov_b32 s87, 0\n"
    ".p2align 3\n"
    "ds_read_b64_tr_b16 a[32:33], v236 offset:8192\n"
    "ds_read_b64_tr_b16 a[34:35], v236 offset:8448\n"
    "ds_read_b64_tr_b16 a[36:37], v236 offset:9216\n"
    "ds_read_b64_tr_b16 a[38:39], v236 offset:9472\n"
    "s_add_u32 m0, s52, s96\n"
    "ds_read_b64_tr_b16 a[40:41], v236 offset:10240\n"
    "buffer_load_dwordx4 v252, s[92:95], s96 offen offset:0 nt lds\n"
    "ds_read_b64_tr_b16 a[42:43], v236 offset:10496\n"
    "buffer_load_dwordx4 v253, s[92:95], s96 offen offset:1024 nt lds\n"
    "ds_read_b64_tr_b16 a[44:45], v236 offset:11264\n"
    "buffer_load_dwordx4 v252, s[92:95], s96 offen offset:2048 nt lds\n"
    "ds_read_b64_tr_b16 a[46:47], v236 offset:11520\n"
    "buffer_load_dwordx4 v253, s[92:95], s96 offen offset:3072 nt lds\n"
    "ds_read_b64_tr_b16 a[48:49], v236 offset:12288\n"
    "ds_read_b64_tr_b16 a[50:51], v236 offset:12544\n"
    "s_add_u32 m0, s52, s97\n"
    "ds_read_b64_tr_b16 a[52:53], v236 offset:13312\n"
    "buffer_load_dwordx4 v252, s[92:95], s97 offen offset:0 nt lds\n"
    "buffer_load_dwordx4 v253, s[92:95], s97 offen offset:1024 nt lds\n"
    "buffer_load_dwordx4 v252, s[92:95], s97 offen offset:2048 nt lds\n"
    "buffer_load_dwordx4 v253, s[92:95], s97 offen offset:3072 nt lds\n"
    "ds_read_b64_tr_b16 a[54:55], v236 offset:13568\n"
    "ds_read_b64_tr_b16 a[56:57], v236 offset:14336\n"
    "ds_read_b64_tr_b16 a[58:59], v236 offset:14592\n"
    "s_waitcnt lgkmcnt(12)\n"
    "ds_read_b64_tr_b16 a[60:61], v236 offset:15360\n"
    "ds_read_b64_tr_b16 a[62:63], v236 offset:15616\n"
    "s_waitcnt lgkmcnt(12)\n"
    "ds_read_b64_tr_b16 a[64:65], v232 offset:16384\n"
    "ds_read_b64_tr_b16 a[66:67], v232 offset:16640\n"
    "s_waitcnt lgkmcnt(12)\n"
    "ds_read_b64_tr_b16 a[68:69], v232 offset:17408\n"
    "ds_read_b64_tr_b16 a[70:71], v232 offset:17664\n"
    "s_waitcnt lgkmcnt(13)\n"
    "ds_read_b64_tr_b16 a[72:73], v232 offset:18432\n"
    "s_waitcnt lgkmcnt(13)\n"
    "ds_read_b64_tr_b16 a[74:75], v232 offset:18688\n"
    "s_waitcnt lgkmcnt(12)\n"
    "ds_read_b64_tr_b16 a[76:77], v232 offset:19456\n"
    "ds_read_b64_tr_b16 a[78:79], v232 offset:19712\n"
    "s_waitcnt lgkmcnt(13)\n"
    "ds_read_b64_tr_b16 a[0:1], v236 offset:0\n"
    "s_waitcnt vmcnt(10)\n"
    "s_waitcnt lgkmcnt(0)\n"
    "ds_read_b128 a[128:131], v243 offset:0\n"
    "ds_read_b128 a[132:135], v244 offset:0\n"
    "ds_read_b128 a[136:139], v245 offset:0\n"
    "ds_read_b128 a[140:143], v246 offset:0\n"
    "ds_read_b64_tr_b16 a[2:3], v236 offset:256\n"
    "ds_read_b64_tr_b16 a[4:5], v236 offset:1024\n"
    "ds_read_b64_tr_b16 a[6:7], v236 offset:1280\n"
    "ds_read_b64_tr_b16 a[8:9], v236 offset:2048\n"
    "ds_read_b64_tr_b16 a[10:11], v236 offset:2304\n"
    "ds_read_b64_tr_b16 a[12:13], v236 offset:3072\n"
    "ds_read_b64_tr_b16 a[14:15], v236 offset:3328\n"
    "ds_read_b64_tr_b16 a[80:81], v232 offset:20480\n"
    "ds_read_b64_tr_b16 a[82:83], v232 offset:20736\n"
    "ds_read_b64_tr_b16 a[84:85], v232 offset:21504\n"
    "s_waitcnt lgkmcnt(13)\n"
    "v_mfma_f32_32x32x16_bf16 v[96:111], a[32:35], a[128:131], 0\n"
    "ds_read_b64_tr_b16 a[86:87], v232 offset:21760\n"
    "s_waitcnt lgkmcnt(13)\n"
    "ds_read_b64_tr_b16 a[88:89], v232 offset:22528\n"
    "s_waitcnt lgkmcnt(12)\n"
    "ds_read_b64_tr_b16 a[90:91], v232 offset:22784\n"
    "v_mfma_f32_32x32x16_bf16 v[96:111], a[36:39], a[132:135], v[96:111]\n"
    "ds_read_b64_tr_b16 a[92:93], v232 offset:23552\n"
    "s_waitcnt lgkmcnt(11)\n"
    "ds_read_b64_tr_b16 a[94:95], v232 offset:23808\n"
    "ds_read_b64_tr_b16 a[16:17], v236 offset:4096\n"
    "v_mfma_f32_32x32x16_bf16 v[96:111], a[40:43], a[136:139], v[96:111]\n"
    "ds_read_b64_tr_b16 a[18:19], v236 offset:4352\n"
    "s_waitcnt lgkmcnt(10)\n"
    "ds_read_b64_tr_b16 a[20:21], v236 offset:5120\n"
    "ds_read_b64_tr_b16 a[22:23], v236 offset:5376\n"
    "v_mfma_f32_32x32x16_bf16 v[96:111], a[44:47], a[140:143], v[96:111]\n"
    "ds_read_b64_tr_b16 a[24:25], v236 offset:6144\n"
    "ds_read_b64_tr_b16 a[26:27], v236 offset:6400\n"
    "s_waitcnt lgkmcnt(11)\n"
    "ds_read_b64_tr_b16 a[28:29], v236 offset:7168\n"
    "v_mfma_f32_32x32x16_bf16 v[112:127], a[0:3], a[128:131], 0\n"
    "ds_read_b64_tr_b16 a[30:31], v236 offset:7424\n"
    "ds_read_b64_tr_b16 a[96:97], v230 offset:24576\n"
    "s_waitcnt lgkmcnt(13)\n"
    "ds_read_b64_tr_b16 a[98:99], v230 offset:25088\n"
    "v_mfma_f32_32x32x16_bf16 v[112:127], a[4:7], a[132:135], v[112:127]\n"
    "s_waitcnt lgkmcnt(13)\n"
    "ds_read_b64_tr_b16 a[100:101], v230 offset:26624\n"
    "s_waitcnt lgkmcnt(13)\n"
    "ds_read_b64_tr_b16 a[102:103], v230 offset:27136\n"
    "s_waitcnt lgkmcnt(13)\n"
    "ds_read_b64_tr_b16 a[104:105], v230 offset:28672\n"
    "v_mfma_f32_32x32x16_bf16 v[112:127], a[8:11], a[136:139], v[112:127]\n"
    "s_waitcnt lgkmcnt(12)\n"
    "ds_read_b64_tr_b16 a[106:107], v230 offset:29184\n"
    "ds_read_b64_tr_b16 a[108:109], v230 offset:30720\n"
    "s_waitcnt lgkmcnt(12)\n"
    "ds_read_b64_tr_b16 a[110:111], v230 offset:31232\n"
    "v_mfma_f32_32x32x16_bf16 v[112:127], a[12:15], a[140:143], v[112:127]\n"
    "ds_read_b64_tr_b16 a[112:113], v230 offset:24640\n"
    "s_waitcnt lgkmcnt(12)\n"
    "ds_read_b64_tr_b16 a[114:115], v230 offset:25152\n"
    "ds_read_b64_tr_b16 a[116:117], v230 offset:26688\n"
    "v_mfma_f32_32x32x16_bf16 v[128:143], a[128:131], a[64:67], 0\n"
    "s_waitcnt lgkmcnt(12)\n"
    "ds_read_b64_tr_b16 a[118:119], v230 offset:27200\n"
    "ds_read_b64_tr_b16 a[120:121], v230 offset:28736\n"
    "s_waitcnt lgkmcnt(12)\n"
    "ds_read_b64_tr_b16 a[122:123], v230 offset:29248\n"
    "v_mfma_f32_32x32x16_bf16 v[128:143], a[132:135], a[68:71], v[128:143]\n"
    "ds_read_b64_tr_b16 a[124:125], v230 offset:30784\n"
    "v_cvt_pk_bf16_f32 v0, v96, v97\n"
    "s_waitcnt lgkmcnt(12)\n"
    "ds_read_b64_tr_b16 a[126:127], v230 offset:31296\n"
    "v_cvt_pk_bf16_f32 v1, v98, v99\n"
    "v_mfma_f32_32x32x16_bf16 v[128:143], a[136:139], a[72:75], v[128:143]\n"
    "s_waitcnt lgkmcnt(11)\n"
    "v_cvt_pk_bf16_f32 v2, v100, v101\n"
    "v_cvt_pk_bf16_f32 v3, v102, v103\n"
    "v_cvt_pk_bf16_f32 v4, v104, v105\n"
    "v_cvt_pk_bf16_f32 v5, v106, v107\n"
    "v_cvt_pk_bf16_f32 v6, v108, v109\n"
    "v_cvt_pk_bf16_f32 v7, v110, v111\n"
    "v_mfma_f32_32x32x16_bf16 v[128:143], a[140:143], a[76:79], v[128:143]\n"
    "s_waitcnt lgkmcnt(8)\n"
    "v_cvt_pk_bf16_f32 v208, v112, v113\n"
    "v_cvt_pk_bf16_f32 v209, v114, v115\n"
    "v_cvt_pk_bf16_f32 v210, v116, v117\n"
    "v_cvt_pk_bf16_f32 v211, v118, v119\n"
    "v_cvt_pk_bf16_f32 v212, v120, v121\n"
    "v_cvt_pk_bf16_f32 v213, v122, v123\n"
    "v_mfma_f32_32x32x16_bf16 v[144:159], v[0:3], v[208:211], 0\n"
    "s_waitcnt lgkmcnt(5)\n"
    "v_cvt_pk_bf16_f32 v214, v124, v125\n"
    "v_cvt_pk_bf16_f32 v215, v126, v127\n"
    "s_nop 0\n"
    "v_cvt_pk_bf16_f32 v48, v128, v129\n"
    "v_cvt_pk_bf16_f32 v49, v130, v131\n"
    "v_cvt_pk_bf16_f32 v50, v132, v133\n"
    "v_mfma_f32_32x32x16_bf16 v[144:159], v[4:7], v[212:215], v[144:159]\n"
    "s_waitcnt lgkmcnt(3)\n"
    "v_cvt_pk_bf16_f32 v51, v134, v135\n"
    "v_cvt_pk_bf16_f32 v52, v136, v137\n"
    "v_cvt_pk_bf16_f32 v53, v138, v139\n"
    "v_cvt_pk_bf16_f32 v54, v140, v141\n"
    "v_cvt_pk_bf16_f32 v55, v142, v143\n"
    "v_mfma_f32_32x32x16_bf16 v[160:175], a[48:51], a[128:131], 0\n"
    "s_waitcnt lgkmcnt(0)\n"
    "s_barrier\n"
    "s_nop 1\n"
    "v_exp_f32_e32 v144, v144\n"
    "v_exp_f32_e64 v145, v145\n"
    "v_mfma_f32_32x32x16_bf16 v[160:175], a[52:55], a[132:135], v[160:175]\n"
    "v_exp_f32_e32 v146, v146\n"
    "v_exp_f32_e32 v147, v147\n"
    "v_exp_f32_e64 v148, v148\n"
    "v_mfma_f32_32x32x16_bf16 v[160:175], a[56:59], a[136:139], v[160:175]\n"
    "v_exp_f32_e32 v149, v149\n"
    "v_exp_f32_e32 v150, v150\n"
    "v_exp_f32_e64 v151, v151\n"
    "v_mfma_f32_32x32x16_bf16 v[160:175], a[60:63], a[140:143], v[160:175]\n"
    "v_cvt_pk_bf16_f32 v144, v144, v145\n"
    "v_cvt_pk_bf16_f32 v145, v146, v147\n"
    "v_cvt_pk_bf16_f32 v146, v148, v149\n"
    "v_cvt_pk_bf16_f32 v147, v150, v151\n"
    "v_exp_f32_e64 v152, v152\n"
    "v_mfma_f32_32x32x16_bf16 v[176:191], a[128:131], a[80:83], 0\n"
    "s_waitcnt vmcnt(8)\n"
    "ds_read_b128 a[192:195], v220 offset:0\n"
    "ds_read_b128 a[196:199], v220 offset:32\n"
    "ds_read_b128 a[200:203], v220 offset:64\n"
    "v_mfma_f32_32x32x16_bf16 v[176:191], a[132:135], a[84:87], v[176:191]\n"
    "ds_read_b128 a[204:207], v220 offset:96\n"
    "ds_read_b128 a[208:211], v222 offset:0\n"
    "ds_read_b128 a[212:215], v222 offset:32\n"
    "ds_read_b128 a[216:219], v222 offset:64\n"
    "v_mfma_f32_32x32x16_bf16 v[176:191], a[136:139], a[88:91], v[176:191]\n"
    "ds_read_b128 a[220:223], v222 offset:96\n"
    "v_cvt_pk_bf16_f32 v24, v160, v161\n"
    "v_cvt_pk_bf16_f32 v25, v162, v163\n"
    "v_cvt_pk_bf16_f32 v26, v164, v165\n"
    "v_cvt_pk_bf16_f32 v27, v166, v167\n"
    "v_cvt_pk_bf16_f32 v28, v168, v169\n"
    "v_mfma_f32_32x32x16_bf16 v[176:191], a[140:143], a[92:95], v[176:191]\n"
    "v_cvt_pk_bf16_f32 v29, v170, v171\n"
    "v_cvt_pk_bf16_f32 v30, v172, v173\n"
    "v_cvt_pk_bf16_f32 v31, v174, v175\n"
    "v_exp_f32_e32 v153, v153\n"
    "v_exp_f32_e32 v154, v154\n"
    "v_exp_f32_e32 v155, v155\n"
    "v_exp_f32_e32 v156, v156\n"
    "s_waitcnt lgkmcnt(7)\n"
    "v_mfma_f32_32x32x16_bf16 v[192:207], a[32:35], a[192:195], 0\n"
    "s_waitcnt lgkmcnt(6)\n"
    "v_exp_f32_e32 v157, v157\n"
    "v_exp_f32_e64 v158, v158\n"
    "v_cvt_pk_bf16_f32 v72, v176, v177\n"
    "v_cvt_pk_bf16_f32 v73, v178, v179\n"
    "v_mfma_f32_32x32x16_bf16 v[192:207], a[36:39], a[196:199], v[192:207]\n"
    "s_waitcnt lgkmcnt(3)\n"
    "v_cvt_pk_bf16_f32 v74, v180, v181\n"
    "v_cvt_pk_bf16_f32 v75, v182, v183\n"
    "v_cvt_pk_bf16_f32 v76, v184, v185\n"
    "v_cvt_pk_bf16_f32 v77, v186, v187\n"
    "v_cvt_pk_bf16_f32 v78, v188, v189\n"
    "v_cvt_pk_bf16_f32 v79, v190, v191\n"
    "v_mfma_f32_32x32x16_bf16 v[192:207], a[40:43], a[200:203], v[192:207]\n"
    "s_waitcnt lgkmcnt(0)\n"
    "v_exp_f32_e64 v159, v159\n"
    "v_cvt_pk_bf16_f32 v148, v152, v153\n"
    "v_cvt_pk_bf16_f32 v149, v154, v155\n"
    "v_cvt_pk_bf16_f32 v150, v156, v157\n"
    "s_add_u32 s92, s40, s39\n"
    "s_addc_u32 s93, s41, 0\n"
    "s_cmp_lt_i32 s37, s36\n"
    "s_cselect_b32 s94, s60, 0\n"
    "v_mfma_f32_32x32x16_bf16 v[192:207], a[44:47], a[204:207], v[192:207]\n"
    "v_cvt_pk_bf16_f32 v151, v158, v159\n"
    "v_mfma_f32_32x32x16_bf16 v[192:207], a[48:51], a[208:211], v[192:207]\n"
    "v_mfma_f32_32x32x16_bf16 v[192:207], a[52:55], a[212:215], v[192:207]\n"
    "v_mfma_f32_32x32x16_bf16 v[192:207], a[56:59], a[216:219], v[192:207]\n"
    "v_mfma_f32_32x32x16_bf16 v[192:207], a[60:63], a[220:223], v[192:207]\n"
    "v_mfma_f32_32x32x16_bf16 v[96:111], a[192:195], a[64:67], 0\n"
    "s_nop 3\n"
    "v_cvt_pk_bf16_f32 v192, v192, v193\n"
    "v_cvt_pk_bf16_f32 v193, v194, v195\n"
    "v_cvt_pk_bf16_f32 v194, v196, v197\n"
    "v_mfma_f32_32x32x16_bf16 v[96:111], a[196:199], a[68:71], v[96:111]\n"
    "v_cvt_pk_bf16_f32 v195, v198, v199\n"
    "v_cvt_pk_bf16_f32 v196, v200, v201\n"
    "v_cvt_pk_bf16_f32 v197, v202, v203\n"
    "v_cvt_pk_bf16_f32 v198, v204, v205\n"
    "v_cvt_pk_bf16_f32 v199, v206, v207\n"
    "ds_write_b128 v228, v[192:195] offset:0\n"
    "v_mfma_f32_32x32x16_bf16 v[96:111], a[200:203], a[72:75], v[96:111]\n"
    "ds_write_b128 v228, v[196:199] offset:32\n"
    "v_mfma_f32_32x32x16_bf16 v[96:111], a[204:207], a[76:79], v[96:111]\n"
    "v_mfma_f32_32x32x16_bf16 v[96:111], a[208:211], a[80:83], v[96:111]\n"
    "v_mfma_f32_32x32x16_bf16 v[96:111], a[212:215], a[84:87], v[96:111]\n"
    "v_mfma_f32_32x32x16_bf16 v[96:111], a[216:219], a[88:91], v[96:111]\n"
    "v_mfma_f32_32x32x16_bf16 v[96:111], a[220:223], a[92:95], v[96:111]\n"
    "v_mfma_f32_32x32x16_bf16 v[112:127], a[0:3], a[192:195], 0\n"
    "s_nop 3\n"
    "v_cvt_pk_bf16_f32 v128, v96, v97\n"
    "v_cvt_pk_bf16_f32 v129, v104, v105\n"
    "v_cvt_pk_bf16_f32 v130, v98, v99\n"
    "v_mfma_f32_32x32x16_bf16 v[112:127], a[4:7], a[196:199], v[112:127]\n"
    "ds_write_b64 v221, v[128:129] offset:1152\n"
    "v_cvt_pk_bf16_f32 v131, v106, v107\n"
    "v_cvt_pk_bf16_f32 v132, v100, v101\n"
    "v_mfma_f32_32x32x16_bf16 v[112:127], a[8:11], a[200:203], v[112:127]\n"
    "ds_write_b64 v221, v[130:131] offset:1408\n"
    "v_cvt_pk_bf16_f32 v133, v108, v109\n"
    "v_cvt_pk_bf16_f32 v134, v102, v103\n"
    "v_mfma_f32_32x32x16_bf16 v[112:127], a[12:15], a[204:207], v[112:127]\n"
    "ds_write_b64 v221, v[132:133] offset:5760\n"
    "v_cvt_pk_bf16_f32 v135, v110, v111\n"
    "ds_write_b64 v221, v[134:135] offset:6016\n"
    "v_mfma_f32_32x32x16_bf16 v[112:127], a[16:19], a[208:211], v[112:127]\n"
    "ds_read_b128 a[176:179], v249 offset:0\n"
    "ds_read_b128 a[180:183], v249 offset:32\n"
    "ds_read_b64 v[224:225], v229 offset:1152\n"
    "s_waitcnt vmcnt(4)\n"
    "v_mfma_f32_32x32x16_bf16 v[112:127], a[20:23], a[212:215], v[112:127]\n"
    "ds_read_b128 a[144:147], v243 offset:4096\n"
    "ds_read_b128 a[148:151], v244 offset:4096\n"
    "ds_read_b128 a[152:155], v245 offset:4096\n"
    "ds_read_b128 a[156:159], v246 offset:4096\n"
    "v_mfma_f32_32x32x16_bf16 v[112:127], a[24:27], a[216:219], v[112:127]\n"
    "ds_read_b128 a[184:187], v249 offset:64\n"
    "s_waitcnt lgkmcnt(10)\n"
    "ds_read_b128 a[188:191], v249 offset:96\n"
    "v_mfma_f32_32x32x16_bf16 v[112:127], a[28:31], a[220:223], v[112:127]\n"
    "v_mfma_f32_32x32x16_bf16 v[160:175], a[96:99], a[192:195], 0\n"
    "s_nop 3\n"
    "v_cvt_pk_bf16_f32 v112, v112, v113\n"
    "v_cvt_pk_bf16_f32 v113, v114, v115\n"
    "v_cvt_pk_bf16_f32 v114, v116, v117\n"
    "v_mfma_f32_32x32x16_bf16 v[160:175], a[100:103], a[196:199], v[160:175]\n"
    "s_waitcnt lgkmcnt(6)\n"
    "v_cvt_pk_bf16_f32 v115, v118, v119\n"
    "v_cvt_pk_bf16_f32 v116, v120, v121\n"
    "v_cvt_pk_bf16_f32 v117, v122, v123\n"
    "v_cvt_pk_bf16_f32 v118, v124, v125\n"
    "v_cvt_pk_bf16_f32 v119, v126, v127\n"
    "ds_write_b128 v228, v[112:115] offset:576\n"
    "v_mfma_f32_32x32x16_bf16 v[160:175], a[104:107], a[200:203], v[160:175]\n"
    "ds_write_b128 v228, v[116:119] offset:608\n"
    "v_mov_b32_e64 v226, v225\n"
    "v_mfma_f32_32x32x16_bf16 v[160:175], a[108:111], a[204:207], v[160:175]\n"
    "s_waitcnt lgkmcnt(2)\n"
    "v_mfma_f32_32x32x16_bf16 v[160:175], a[112:115], a[208:211], v[160:175]\n"
    "v_mfma_f32_32x32x16_bf16 v[160:175], a[116:119], a[212:215], v[160:175]\n"
    "v_mfma_f32_32x32x16_bf16 v[160:175], a[120:123], a[216:219], v[160:175]\n"
    "v_mfma_f32_32x32x16_bf16 v[160:175], a[124:127], a[220:223], v[160:175]\n"
    "v_mfma_f32_32x32x16_bf16 v[176:191], a[32:35], a[144:147], 0\n"
    "s_nop 3\n"
    "v_cvt_pk_bf16_f32 v160, v160, v161\n"
    "v_cvt_pk_bf16_f32 v161, v162, v163\n"
    "v_cvt_pk_bf16_f32 v162, v164, v165\n"
    "v_mfma_f32_32x32x16_bf16 v[176:191], a[36:39], a[148:151], v[176:191]\n"
    "v_cvt_pk_bf16_f32 v163, v166, v167\n"
    "v_cvt_pk_bf16_f32 v164, v168, v169\n"
    "v_cvt_pk_bf16_f32 v165, v170, v171\n"
    "v_cvt_pk_bf16_f32 v166, v172, v173\n"
    "v_cvt_pk_bf16_f32 v167, v174, v175\n"
    "ds_write_b128 v228, v[160:163] offset:1728\n"
    "v_mfma_f32_32x32x16_bf16 v[176:191], a[40:43], a[152:155], v[176:191]\n"
    "ds_write_b128 v228, v[164:167] offset:1760\n"
    "ds_read_b128 a[224:227], v247 offset:576\n"
    "v_mfma_f32_32x32x16_bf16 v[176:191], a[44:47], a[156:159], v[176:191]\n"
    "ds_read_b128 a[228:231], v247 offset:608\n"
    "ds_read_b128 a[232:235], v248 offset:640\n"
    "ds_read_b128 a[236:239], v248 offset:672\n"
    "v_mfma_f32_32x32x16_bf16 v[192:207], a[144:147], a[64:67], 0\n"
    "s_nop 3\n"
    "v_cvt_pk_bf16_f32 v8, v176, v177\n"
    "v_cvt_pk_bf16_f32 v9, v178, v179\n"
    "v_cvt_pk_bf16_f32 v10, v180, v181\n"
    "v_mfma_f32_32x32x16_bf16 v[192:207], a[148:151], a[68:71], v[192:207]\n"
    "v_cvt_pk_bf16_f32 v11, v182, v183\n"
    "v_cvt_pk_bf16_f32 v12, v184, v185\n"
    "v_cvt_pk_bf16_f32 v13, v186, v187\n"
    "v_cvt_pk_bf16_f32 v14, v188, v189\n"
    "v_cvt_pk_bf16_f32 v15, v190, v191\n"
    "v_mfma_f32_32x32x16_bf16 v[192:207], a[152:155], a[72:75], v[192:207]\n"
    "v_mfma_f32_32x32x16_bf16 v[192:207], a[156:159], a[76:79], v[192:207]\n"
    "v_mfma_f32_16x16x32_bf16 v[216:219], a[176:179], v[208:211], 0\n"
    "s_waitcnt lgkmcnt(0)\n"
    "v_mfma_f32_16x16x32_bf16 v[216:219], a[180:183], v[212:215], v[216:219]\n"
    "v_cvt_pk_bf16_f32 v56, v192, v193\n"
    "v_cvt_pk_bf16_f32 v57, v194, v195\n"
    "v_mfma_f32_32x32x16_bf16 v[128:143], v[8:11], v[208:211], 0\n"
    "v_cvt_pk_bf16_f32 v58, v196, v197\n"
    "v_cvt_pk_bf16_f32 v59, v198, v199\n"
    "v_cvt_pk_bf16_f32 v60, v200, v201\n"
    "v_cvt_pk_bf16_f32 v61, v202, v203\n"
    "v_cvt_pk_bf16_f32 v62, v204, v205\n"
    "v_cvt_pk_bf16_f32 v63, v206, v207\n"
    "v_mfma_f32_32x32x16_bf16 v[128:143], v[12:15], v[212:215], v[128:143]\n"
    "v_exp_f32_e32 v216, v216\n"
    "v_exp_f32_e64 v217, v217\n"
    "s_nop 0\n"
    "v_cvt_pk_bf16_f32 v230, v216, v217\n"
    "v_mfma_f32_32x32x16_bf16 v[96:111], a[48:51], a[144:147], 0\n"
    "s_nop 3\n"
    "v_exp_f32_e32 v128, v128\n"
    "v_exp_f32_e64 v129, v129\n"
    "v_mfma_f32_32x32x16_bf16 v[96:111], a[52:55], a[148:151], v[96:111]\n"
    "v_exp_f32_e32 v130, v130\n"
    "v_exp_f32_e32 v131, v131\n"
    "v_exp_f32_e64 v132, v132\n"
    "v_mfma_f32_32x32x16_bf16 v[96:111], a[56:59], a[152:155], v[96:111]\n"
    "v_exp_f32_e32 v133, v133\n"
    "v_exp_f32_e32 v134, v134\n"
    "v_exp_f32_e64 v135, v135\n"
    "v_mfma_f32_32x32x16_bf16 v[96:111], a[60:63], a[156:159], v[96:111]\n"
    "v_cvt_pk_bf16_f32 v128, v128, v129\n"
    "v_cvt_pk_bf16_f32 v129, v130, v131\n"
    "v_cvt_pk_bf16_f32 v130, v132, v133\n"
    "v_cvt_pk_bf16_f32 v131, v134, v135\n"
    "v_exp_f32_e64 v136, v136\n"
    "v_mfma_f32_16x16x16_bf16 v[220:223], v[234:235], v[230:231], 0\n"
    "v_exp_f32_e32 v137, v137\n"
    "v_exp_f32_e32 v138, v138\n"
    "v_mfma_f32_32x32x8_bf16 v[112:127], v[224:225], v[230:231], 0\n"
    "v_cvt_pk_bf16_f32 v32, v96, v97\n"
    "v_cvt_pk_bf16_f32 v33, v98, v99\n"
    "v_cvt_pk_bf16_f32 v34, v100, v101\n"
    "s_waitcnt vmcnt(0)\n"
    "ds_read_b128 a[160:163], v243 offset:8192\n"
    "v_add_u32_e32 v243, s86, v243\n"
    "v_smfmac_f32_16x16x64_bf16 v[220:223], v[238:241], v[144:151], v235\n"
    "ds_read_b128 a[164:167], v244 offset:8192\n"
    "v_add_u32_e64 v244, s86, v244\n"
    "v_mfma_f32_32x32x16_bf16 v[112:127], v[48:51], v[144:147], v[112:127]\n"
    "ds_read_b128 a[168:171], v245 offset:8192\n"
    "v_add_u32_e64 v245, s86, v245\n"
    "ds_read_b128 a[172:175], v246 offset:8192\n"
    "v_add_u32_e64 v246, s86, v246\n"
    "v_cvt_pk_bf16_f32 v35, v102, v103\n"
    "v_mfma_f32_32x32x16_bf16 v[112:127], v[52:55], v[148:151], v[112:127]\n"
    "v_cvt_pk_bf16_f32 v36, v104, v105\n"
    "v_cvt_pk_bf16_f32 v37, v106, v107\n"
    "v_cvt_pk_bf16_f32 v38, v108, v109\n"
    "v_cvt_pk_bf16_f32 v39, v110, v111\n"
    "v_exp_f32_e64 v139, v139\n"
    "v_mfma_f32_32x32x16_bf16 v[160:175], a[96:99], a[128:131], 0\n"
    "v_exp_f32_e32 v140, v140\n"
    "v_exp_f32_e32 v141, v141\n"
    "v_exp_f32_e64 v142, v142\n"
    "v_mfma_f32_32x32x16_bf16 v[160:175], a[100:103], a[132:135], v[160:175]\n"
    "s_add_u32 m0, s35, 0\n"
    "v_exp_f32_e32 v143, v143\n"
    "buffer_load_dwordx4 v252, s[88:91], 0 offen offset:0 nt lds\n"
    "v_cvt_pk_bf16_f32 v132, v136, v137\n"
    "v_cvt_pk_bf16_f32 v133, v138, v139\n"
    "v_cvt_pk_bf16_f32 v134, v140, v141\n"
    "v_mfma_f32_32x32x16_bf16 v[160:175], a[104:107], a[136:139], v[160:175]\n"
    "s_waitcnt lgkmcnt(2)\n"
    "v_cvt_pk_bf16_f32 v135, v142, v143\n"
    "v_mfma_f32_32x32x16_bf16 v[160:175], a[108:111], a[140:143], v[160:175]\n"
    "s_waitcnt lgkmcnt(0)\n"
    "v_mfma_f32_32x32x16_bf16 v[176:191], a[32:35], a[160:163], 0\n"
    "v_mfma_f32_32x32x16_bf16 v[176:191], a[36:39], a[164:167], v[176:191]\n"
    "v_mfma_f32_32x32x16_bf16 v[176:191], a[40:43], a[168:171], v[176:191]\n"
    "v_mfma_f32_32x32x16_bf16 v[176:191], a[44:47], a[172:175], v[176:191]\n"
    "buffer_load_dwordx4 v253, s[88:91], 0 offen offset:1024 nt lds\n"
    "v_mfma_f32_32x32x16_bf16 v[192:207], a[160:163], a[64:67], 0\n"
    "s_nop 3\n"
    "v_cvt_pk_bf16_f32 v16, v176, v177\n"
    "v_cvt_pk_bf16_f32 v17, v178, v179\n"
    "v_cvt_pk_bf16_f32 v18, v180, v181\n"
    "v_mfma_f32_32x32x16_bf16 v[192:207], a[164:167], a[68:71], v[192:207]\n"
    "v_cvt_pk_bf16_f32 v19, v182, v183\n"
    "v_cvt_pk_bf16_f32 v20, v184, v185\n"
    "v_cvt_pk_bf16_f32 v21, v186, v187\n"
    "v_cvt_pk_bf16_f32 v22, v188, v189\n"
    "v_cvt_pk_bf16_f32 v23, v190, v191\n"
    "v_mfma_f32_32x32x16_bf16 v[192:207], a[168:171], a[72:75], v[192:207]\n"
    "v_mfma_f32_32x32x16_bf16 v[192:207], a[172:175], a[76:79], v[192:207]\n"
    "v_mfma_f32_32x32x16_bf16 v[96:111], v[16:19], v[208:211], 0\n"
    "s_nop 3\n"
    "v_cvt_pk_bf16_f32 v64, v192, v193\n"
    "v_cvt_pk_bf16_f32 v65, v194, v195\n"
    "v_cvt_pk_bf16_f32 v66, v196, v197\n"
    "v_mfma_f32_32x32x16_bf16 v[96:111], v[20:23], v[212:215], v[96:111]\n"
    "v_cvt_pk_bf16_f32 v67, v198, v199\n"
    "v_cvt_pk_bf16_f32 v68, v200, v201\n"
    "v_cvt_pk_bf16_f32 v69, v202, v203\n"
    "v_cvt_pk_bf16_f32 v70, v204, v205\n"
    "v_cvt_pk_bf16_f32 v71, v206, v207\n"
    "v_mfma_f32_32x32x16_bf16 v[112:127], v[56:59], v[128:131], v[112:127]\n"
    "buffer_load_dwordx4 v252, s[88:91], 0 offen offset:2048 nt lds\n"
    "s_nop 2\n"
    "v_exp_f32_e32 v96, v96\n"
    "v_exp_f32_e64 v97, v97\n"
    "v_mfma_f32_32x32x16_bf16 v[112:127], v[60:63], v[132:135], v[112:127]\n"
    "v_exp_f32_e32 v98, v98\n"
    "v_exp_f32_e32 v99, v99\n"
    "v_exp_f32_e64 v100, v100\n"
    "v_smfmac_f32_16x16x64_bf16 v[220:223], v[238:241], v[128:135], v235\n"
    "v_exp_f32_e64 v101, v101\n"
    "v_mfma_f32_32x32x16_bf16 v[144:159], a[0:3], a[144:147], 0\n"
    "v_exp_f32_e32 v102, v102\n"
    "v_exp_f32_e32 v103, v103\n"
    "v_cvt_pk_bf16_f32 v96, v96, v97\n"
    "v_cvt_pk_bf16_f32 v97, v98, v99\n"
    "v_mfma_f32_32x32x16_bf16 v[144:159], a[4:7], a[148:151], v[144:159]\n"
    "v_cvt_pk_bf16_f32 v98, v100, v101\n"
    "v_cvt_pk_bf16_f32 v99, v102, v103\n"
    "v_exp_f32_e32 v104, v104\n"
    "v_exp_f32_e32 v105, v105\n"
    "v_mfma_f32_32x32x16_bf16 v[144:159], a[8:11], a[152:155], v[144:159]\n"
    "v_exp_f32_e32 v106, v106\n"
    "v_exp_f32_e32 v107, v107\n"
    "v_exp_f32_e64 v108, v108\n"
    "v_mfma_f32_32x32x16_bf16 v[144:159], a[12:15], a[156:159], v[144:159]\n"
    "v_exp_f32_e32 v109, v109\n"
    "v_exp_f32_e32 v110, v110\n"
    "v_exp_f32_e64 v111, v111\n"
    "v_mfma_f32_32x32x16_bf16 v[112:127], v[64:67], v[96:99], v[112:127]\n"
    "buffer_load_dwordx4 v253, s[88:91], 0 offen offset:3072 nt lds\n"
    "v_cvt_pk_bf16_f32 v100, v104, v105\n"
    "v_cvt_pk_bf16_f32 v101, v106, v107\n"
    "v_cvt_pk_bf16_f32 v102, v108, v109\n"
    "v_cvt_pk_bf16_f32 v208, v144, v145\n"
    "v_cvt_pk_bf16_f32 v209, v146, v147\n"
    "v_cvt_pk_bf16_f32 v210, v148, v149\n"
    "v_mfma_f32_32x32x16_bf16 v[128:143], a[48:51], a[160:163], 0\n"
    "v_cvt_pk_bf16_f32 v211, v150, v151\n"
    "v_cvt_pk_bf16_f32 v212, v152, v153\n"
    "v_cvt_pk_bf16_f32 v213, v154, v155\n"
    "v_cvt_pk_bf16_f32 v214, v156, v157\n"
    "v_cvt_pk_bf16_f32 v215, v158, v159\n"
    "v_cvt_pk_bf16_f32 v103, v110, v111\n"
    "v_mfma_f32_32x32x16_bf16 v[176:191], v[0:3], v[208:211], 0\n"
    "v_mfma_f32_32x32x16_bf16 v[176:191], v[4:7], v[212:215], v[176:191]\n"
    "v_mfma_f32_16x16x32_bf16 v[216:219], a[176:179], v[208:211], 0\n"
    "v_mfma_f32_16x16x32_bf16 v[216:219], a[180:183], v[212:215], v[216:219]\n"
    "v_exp_f32_e64 v176, v176\n"
    "v_mfma_f32_32x32x16_bf16 v[112:127], v[68:71], v[100:103], v[112:127]\n"
    "v_exp_f32_e32 v177, v177\n"
    "v_exp_f32_e32 v178, v178\n"
    "v_exp_f32_e64 v179, v179\n"
    "v_smfmac_f32_16x16x64_bf16 v[220:223], v[238:241], v[96:103], v235\n"
    "v_exp_f32_e64 v216, v216\n"
    "v_mfma_f32_32x32x16_bf16 v[192:207], v[8:11], v[208:211], 0\n"
    "v_exp_f32_e32 v217, v217\n"
    "s_add_u32 m0, s35, s96\n"
    "v_exp_f32_e64 v180, v180\n"
    "buffer_load_dwordx4 v252, s[88:91], s96 offen offset:0 nt lds\n"
    "v_mfma_f32_32x32x16_bf16 v[192:207], v[12:15], v[212:215], v[192:207]\n"
    "v_exp_f32_e32 v181, v181\n"
    "v_exp_f32_e32 v182, v182\n"
    "v_exp_f32_e64 v183, v183\n"
    "v_mfma_f32_32x32x16_bf16 v[128:143], a[52:55], a[164:167], v[128:143]\n"
    "v_cvt_pk_bf16_f32 v176, v176, v177\n"
    "v_cvt_pk_bf16_f32 v177, v178, v179\n"
    "v_cvt_pk_bf16_f32 v178, v180, v181\n"
    "v_cvt_pk_bf16_f32 v179, v182, v183\n"
    "v_add_f32_e32 v236, v220, v221\n"
    "v_rcp_f32_e32 v232, v236\n"
    "v_mfma_f32_32x32x16_bf16 v[128:143], a[56:59], a[168:171], v[128:143]\n"
    "v_fmac_f32_e32 v160, v112, v232\n"
    "v_fmac_f32_e32 v161, v113, v232\n"
    "v_fmac_f32_e32 v162, v114, v232\n"
    "v_fmac_f32_e32 v163, v115, v232\n"
    "v_fmac_f32_e32 v164, v116, v232\n"
    "v_fmac_f32_e32 v165, v117, v232\n"
    "v_mfma_f32_32x32x16_bf16 v[128:143], a[60:63], a[172:175], v[128:143]\n"
    "v_fmac_f32_e32 v166, v118, v232\n"
    "v_fmac_f32_e32 v167, v119, v232\n"
    "v_fmac_f32_e32 v168, v120, v232\n"
    "v_fmac_f32_e32 v169, v121, v232\n"
    "v_fmac_f32_e32 v170, v122, v232\n"
    "v_fmac_f32_e32 v171, v123, v232\n"
    "v_mfma_f32_32x32x16_bf16 v[96:111], a[96:99], a[144:147], 0\n"
    "v_fmac_f32_e32 v172, v124, v232\n"
    "v_fmac_f32_e32 v173, v125, v232\n"
    "v_fmac_f32_e32 v174, v126, v232\n"
    "v_fmac_f32_e32 v175, v127, v232\n"
    "v_cvt_pk_bf16_f32 v40, v128, v129\n"
    "v_cvt_pk_bf16_f32 v41, v130, v131\n"
    "v_mfma_f32_32x32x16_bf16 v[96:111], a[100:103], a[148:151], v[96:111]\n"
    "v_cvt_pk_bf16_f32 v42, v132, v133\n"
    "v_cvt_pk_bf16_f32 v43, v134, v135\n"
    "v_cvt_pk_bf16_f32 v44, v136, v137\n"
    "buffer_load_dwordx4 v253, s[88:91], s96 offen offset:1024 nt lds\n"
    "v_cvt_pk_bf16_f32 v45, v138, v139\n"
    "v_cvt_pk_bf16_f32 v46, v140, v141\n"
    "v_mfma_f32_32x32x16_bf16 v[96:111], a[104:107], a[152:155], v[96:111]\n"
    "v_cvt_pk_bf16_f32 v47, v142, v143\n"
    "v_maximum3_f32 v233, v233, v236, v232\n"
    "v_cvt_pk_bf16_f32 v230, v216, v217\n"
    "v_cvt_pk_bf16_f32 v160, v160, v161\n"
    "v_cvt_pk_bf16_f32 v161, v162, v163\n"
    "v_cvt_pk_bf16_f32 v162, v164, v165\n"
    "v_mfma_f32_16x16x16_bf16 v[220:223], v[234:235], v[230:231], 0\n"
    "v_cvt_pk_bf16_f32 v163, v166, v167\n"
    "v_pk_max_i16 v160, v160, 0\n"
    "v_pk_max_i16 v161, v161, 0\n"
    "v_mfma_f32_32x32x8_bf16 v[144:159], v[224:225], v[230:231], 0\n"
    "v_pk_max_i16 v162, v162, 0\n"
    "v_pk_max_i16 v163, v163, 0\n"
    "v_cvt_pk_bf16_f32 v168, v168, v169\n"
    "v_cvt_pk_bf16_f32 v169, v170, v171\n"
    "v_cvt_pk_bf16_f32 v170, v172, v173\n"
    "v_cvt_pk_bf16_f32 v171, v174, v175\n"
    "v_pk_max_i16 v168, v168, 0\n"
    "v_mfma_f32_32x32x16_bf16 v[96:111], a[108:111], a[156:159], v[96:111]\n"
    "ds_write_b128 v251, v[160:163] offset:23552\n"
    "v_pk_max_i16 v169, v169, 0\n"
    "v_pk_max_i16 v170, v170, 0\n"
    "v_mfma_f32_32x32x16_bf16 v[144:159], v[48:51], v[176:179], v[144:159]\n"
    "v_pk_max_i16 v171, v171, 0\n"
    "v_exp_f32_e32 v184, v184\n"
    "v_exp_f32_e32 v185, v185\n"
    "v_exp_f32_e64 v186, v186\n"
    "v_mfma_f32_32x32x16_bf16 v[128:143], a[144:147], a[80:83], 0\n"
    "ds_write_b128 v251, v[168:171] offset:24576\n"
    "v_exp_f32_e64 v187, v187\n"
    "v_mfma_f32_32x32x16_bf16 v[128:143], a[148:151], a[84:87], v[128:143]\n"
    "v_exp_f32_e64 v188, v188\n"
    "buffer_load_dwordx4 v252, s[88:91], s96 offen offset:2048 nt lds\n"
    "v_exp_f32_e32 v189, v189\n"
    "v_exp_f32_e32 v190, v190\n"
    "v_mfma_f32_32x32x16_bf16 v[128:143], a[152:155], a[88:91], v[128:143]\n"
    "v_exp_f32_e64 v191, v191\n"
    "v_cvt_pk_bf16_f32 v180, v184, v185\n"
    "v_cvt_pk_bf16_f32 v181, v186, v187\n"
    "v_cvt_pk_bf16_f32 v182, v188, v189\n"
    "v_cvt_pk_bf16_f32 v183, v190, v191\n"
    "v_mfma_f32_32x32x16_bf16 v[128:143], a[156:159], a[92:95], v[128:143]\n"
    "v_exp_f32_e32 v192, v192\n"
    "v_exp_f32_e32 v193, v193\n"
    "v_exp_f32_e64 v194, v194\n"
    "v_mfma_f32_32x32x16_bf16 v[144:159], v[52:55], v[180:183], v[144:159]\n"
    "v_exp_f32_e32 v195, v195\n"
    "v_exp_f32_e32 v196, v196\n"
    "v_exp_f32_e64 v197, v197\n"
    "v_smfmac_f32_16x16x64_bf16 v[220:223], v[238:241], v[176:183], v235\n"
    "v_cvt_pk_bf16_f32 v80, v128, v129\n"
    "v_mfma_f32_32x32x16_bf16 v[112:127], v[16:19], v[208:211], 0\n"
    "v_cvt_pk_bf16_f32 v81, v130, v131\n"
    "v_cvt_pk_bf16_f32 v82, v132, v133\n"
    "v_cvt_pk_bf16_f32 v83, v134, v135\n"
    "v_cvt_pk_bf16_f32 v84, v136, v137\n"
    "v_cvt_pk_bf16_f32 v85, v138, v139\n"
    "v_cvt_pk_bf16_f32 v86, v140, v141\n"
    "v_mfma_f32_32x32x16_bf16 v[112:127], v[20:23], v[212:215], v[112:127]\n"
    "v_cvt_pk_bf16_f32 v87, v142, v143\n"
    "v_exp_f32_e32 v198, v198\n"
    "v_exp_f32_e32 v199, v199\n"
    "v_cvt_pk_bf16_f32 v192, v192, v193\n"
    "v_mfma_f32_32x32x16_bf16 v[160:175], a[0:3], a[160:163], 0\n"
    "v_cvt_pk_bf16_f32 v193, v194, v195\n"
    "buffer_load_dwordx4 v253, s[88:91], s96 offen offset:3072 nt lds\n"
    "v_cvt_pk_bf16_f32 v194, v196, v197\n"
    "v_cvt_pk_bf16_f32 v195, v198, v199\n"
    "v_exp_f32_e64 v200, v200\n"
    "v_mfma_f32_32x32x16_bf16 v[160:175], a[4:7], a[164:167], v[160:175]\n"
    "v_exp_f32_e32 v201, v201\n"
    "v_exp_f32_e32 v202, v202\n"
    "v_exp_f32_e64 v203, v203\n"
    "v_mfma_f32_32x32x16_bf16 v[144:159], v[56:59], v[192:195], v[144:159]\n"
    "v_exp_f32_e32 v204, v204\n"
    "v_exp_f32_e32 v205, v205\n"
    "v_exp_f32_e64 v206, v206\n"
    "v_mfma_f32_32x32x16_bf16 v[160:175], a[8:11], a[168:171], v[160:175]\n"
    "v_exp_f32_e64 v207, v207\n"
    "v_cvt_pk_bf16_f32 v196, v200, v201\n"
    "v_cvt_pk_bf16_f32 v197, v202, v203\n"
    "v_cvt_pk_bf16_f32 v198, v204, v205\n"
    "v_cvt_pk_bf16_f32 v199, v206, v207\n"
    "v_mfma_f32_32x32x16_bf16 v[160:175], a[12:15], a[172:175], v[160:175]\n"
    "v_exp_f32_e32 v112, v112\n"
    "v_exp_f32_e32 v113, v113\n"
    "v_exp_f32_e64 v114, v114\n"
    "v_mfma_f32_32x32x16_bf16 v[144:159], v[60:63], v[196:199], v[144:159]\n"
    "v_exp_f32_e32 v115, v115\n"
    "v_exp_f32_e32 v116, v116\n"
    "v_exp_f32_e64 v117, v117\n"
    "v_smfmac_f32_16x16x64_bf16 v[220:223], v[238:241], v[192:199], v235\n"
    "v_exp_f32_e64 v118, v118\n"
    "v_mfma_f32_16x16x32_bf16 v[192:195], v[0:3], a[224:227], 0\n"
    "v_exp_f32_e64 v119, v119\n"
    "v_mfma_f32_16x16x32_bf16 v[196:199], v[8:11], a[224:227], 0\n"
    "v_cvt_pk_bf16_f32 v112, v112, v113\n"
    "s_add_u32 m0, s52, s97\n"
    "v_mfma_f32_16x16x32_bf16 v[200:203], v[16:19], a[224:227], 0\n"
    "buffer_load_dwordx4 v252, s[88:91], s97 offen offset:0 nt lds\n"
    "v_cvt_pk_bf16_f32 v113, v114, v115\n"
    "v_cvt_pk_bf16_f32 v114, v116, v117\n"
    "v_mfma_f32_16x16x32_bf16 v[204:207], a[176:179], a[224:227], 0\n"
    "v_cvt_pk_bf16_f32 v115, v118, v119\n"
    "v_cvt_pk_bf16_f32 v208, v160, v161\n"
    "v_mfma_f32_16x16x32_bf16 v[192:195], v[4:7], a[228:231], v[192:195]\n"
    "v_cvt_pk_bf16_f32 v209, v162, v163\n"
    "v_cvt_pk_bf16_f32 v210, v164, v165\n"
    "v_mfma_f32_32x32x16_bf16 v[144:159], v[64:67], v[112:115], v[144:159]\n"
    "v_cvt_pk_bf16_f32 v211, v166, v167\n"
    "v_cvt_pk_bf16_f32 v212, v168, v169\n"
    "v_cvt_pk_bf16_f32 v213, v170, v171\n"
    "v_cvt_pk_bf16_f32 v214, v172, v173\n"
    "v_cvt_pk_bf16_f32 v215, v174, v175\n"
    "v_exp_f32_e32 v120, v120\n"
    "v_mfma_f32_32x32x16_bf16 v[176:191], v[0:3], v[208:211], 0\n"
    "v_exp_f32_e32 v121, v121\n"
    "v_exp_f32_e32 v122, v122\n"
    "v_exp_f32_e64 v123, v123\n"
    "v_mfma_f32_32x32x16_bf16 v[176:191], v[4:7], v[212:215], v[176:191]\n"
    "v_exp_f32_e32 v124, v124\n"
    "v_exp_f32_e32 v125, v125\n"
    "v_exp_f32_e64 v126, v126\n"
    "v_mfma_f32_16x16x32_bf16 v[216:219], a[176:179], v[208:211], 0\n"
    "v_exp_f32_e64 v127, v127\n"
    "v_mfma_f32_16x16x32_bf16 v[216:219], a[180:183], v[212:215], v[216:219]\n"
    "v_cvt_pk_bf16_f32 v116, v120, v121\n"
    "v_cvt_pk_bf16_f32 v117, v122, v123\n"
    "v_mfma_f32_32x32x16_bf16 v[128:143], v[8:11], v[208:211], 0\n"
    "v_cvt_pk_bf16_f32 v118, v124, v125\n"
    "v_cvt_pk_bf16_f32 v119, v126, v127\n"
    "v_exp_f32_e64 v176, v176\n"
    "buffer_load_dwordx4 v253, s[88:91], s97 offen offset:1024 nt lds\n"
    "v_exp_f32_e64 v216, v216\n"
    "v_mfma_f32_32x32x16_bf16 v[144:159], v[68:71], v[116:119], v[144:159]\n"
    "v_exp_f32_e32 v217, v217\n"
    "v_exp_f32_e32 v177, v177\n"
    "v_exp_f32_e64 v178, v178\n"
    "v_smfmac_f32_16x16x64_bf16 v[220:223], v[238:241], v[112:119], v235\n"
    "v_exp_f32_e64 v179, v179\n"
    "v_mfma_f32_32x32x16_bf16 v[128:143], v[12:15], v[212:215], v[128:143]\n"
    "v_exp_f32_e32 v180, v180\n"
    "v_exp_f32_e32 v181, v181\n"
    "v_exp_f32_e64 v182, v182\n"
    "v_mfma_f32_16x16x32_bf16 v[196:199], v[12:15], a[228:231], v[196:199]\n"
    "v_exp_f32_e64 v183, v183\n"
    "v_mfma_f32_16x16x32_bf16 v[200:203], v[20:23], a[228:231], v[200:203]\n"
    "v_cvt_pk_bf16_f32 v176, v176, v177\n"
    "v_cvt_pk_bf16_f32 v177, v178, v179\n"
    "v_mfma_f32_16x16x32_bf16 v[204:207], a[180:183], a[228:231], v[204:207]\n"
    "v_cvt_pk_bf16_f32 v178, v180, v181\n"
    "v_cvt_pk_bf16_f32 v179, v182, v183\n"
    "v_mfma_f32_16x16x32_bf16 v[192:195], v[24:27], a[232:235], v[192:195]\n"
    "v_add_f32_e32 v236, v220, v221\n"
    "v_rcp_f32_e32 v232, v236\n"
    "v_mfma_f32_16x16x32_bf16 v[196:199], v[32:35], a[232:235], v[196:199]\n"
    "v_fmac_f32_e32 v96, v144, v232\n"
    "v_fmac_f32_e32 v97, v145, v232\n"
    "v_mfma_f32_16x16x32_bf16 v[200:203], v[40:43], a[232:235], v[200:203]\n"
    "v_fmac_f32_e32 v98, v146, v232\n"
    "v_fmac_f32_e32 v99, v147, v232\n"
    "v_mfma_f32_16x16x32_bf16 v[204:207], a[184:187], a[232:235], v[204:207]\n"
    "v_fmac_f32_e64 v100, v148, v232\n"
    "buffer_load_dwordx4 v252, s[88:91], s97 offen offset:2048 nt lds\n"
    "v_mfma_f32_16x16x32_bf16 v[192:195], v[28:31], a[236:239], v[192:195]\n"
    "v_fmac_f32_e32 v101, v149, v232\n"
    "v_fmac_f32_e32 v102, v150, v232\n"
    "v_mfma_f32_16x16x32_bf16 v[196:199], v[36:39], a[236:239], v[196:199]\n"
    "v_fmac_f32_e32 v103, v151, v232\n"
    "v_fmac_f32_e32 v104, v152, v232\n"
    "v_mfma_f32_16x16x32_bf16 v[200:203], v[44:47], a[236:239], v[200:203]\n"
    "v_fmac_f32_e32 v105, v153, v232\n"
    "v_fmac_f32_e32 v106, v154, v232\n"
    "v_mfma_f32_16x16x32_bf16 v[204:207], a[188:191], a[236:239], v[204:207]\n"
    "v_fmac_f32_e32 v107, v155, v232\n"
    "v_fmac_f32_e32 v108, v156, v232\n"
    "v_mfma_f32_32x32x16_bf16 v[160:175], a[96:99], a[160:163], 0\n"
    "v_fmac_f32_e32 v109, v157, v232\n"
    "v_fmac_f32_e32 v110, v158, v232\n"
    "v_fmac_f32_e64 v111, v159, v232\n"
    "v_maximum3_f32 v233, v233, v236, v232\n"
    "v_cvt_pk_bf16_f32 v230, v216, v217\n"
    "v_cvt_pk_bf16_f32 v96, v96, v97\n"
    "v_mfma_f32_32x32x16_bf16 v[160:175], a[100:103], a[164:167], v[160:175]\n"
    "v_cvt_pk_bf16_f32 v97, v98, v99\n"
    "v_cvt_pk_bf16_f32 v98, v100, v101\n"
    "v_cvt_pk_bf16_f32 v99, v102, v103\n"
    "v_pk_max_i16 v96, v96, 0\n"
    "v_pk_max_i16 v97, v97, 0\n"
    "v_pk_max_i16 v98, v98, 0\n"
    "v_mfma_f32_16x16x16_bf16 v[220:223], v[234:235], v[230:231], 0\n"
    "v_pk_max_i16 v99, v99, 0\n"
    "v_cvt_pk_bf16_f32 v104, v104, v105\n"
    "v_cvt_pk_bf16_f32 v105, v106, v107\n"
    "v_mfma_f32_32x32x8_bf16 v[112:127], v[224:225], v[230:231], 0\n"
    "v_cvt_pk_bf16_f32 v106, v108, v109\n"
    "v_cvt_pk_bf16_f32 v107, v110, v111\n"
    "v_pk_max_i16 v104, v104, 0\n"
    "v_pk_max_i16 v105, v105, 0\n"
    "v_pk_max_i16 v106, v106, 0\n"
    "v_pk_max_i16 v107, v107, 0\n"
    "v_exp_f32_e64 v184, v184\n"
    "v_mfma_f32_32x32x16_bf16 v[160:175], a[104:107], a[168:171], v[160:175]\n"
    "ds_write_b128 v251, v[96:99] offset:25600\n"
    "buffer_load_dwordx4 v253, s[88:91], s97 offen offset:3072 nt lds\n"
    "v_exp_f32_e64 v185, v185\n"
    "v_mfma_f32_32x32x16_bf16 v[112:127], v[48:51], v[176:179], v[112:127]\n"
    "ds_write_b128 v251, v[104:107] offset:26624\n"
    "v_exp_f32_e64 v186, v186\n"
    "v_mfma_f32_32x32x16_bf16 v[160:175], a[108:111], a[172:175], v[160:175]\n"
    "v_exp_f32_e32 v187, v187\n"
    "v_exp_f32_e32 v188, v188\n"
    "v_exp_f32_e64 v189, v189\n"
    "v_mfma_f32_32x32x16_bf16 v[144:159], a[160:163], a[80:83], 0\n"
    "v_exp_f32_e32 v190, v190\n"
    "v_exp_f32_e32 v191, v191\n"
    "v_cvt_pk_bf16_f32 v180, v184, v185\n"
    "v_cvt_pk_bf16_f32 v181, v186, v187\n"
    "v_mfma_f32_32x32x16_bf16 v[144:159], a[164:167], a[84:87], v[144:159]\n"
    "v_cvt_pk_bf16_f32 v182, v188, v189\n"
    "v_cvt_pk_bf16_f32 v183, v190, v191\n"
    "v_exp_f32_e32 v192, v192\n"
    "v_exp_f32_e32 v193, v193\n"
    "v_mfma_f32_32x32x16_bf16 v[112:127], v[52:55], v[180:183], v[112:127]\n"
    "v_exp_f32_e32 v194, v194\n"
    "v_exp_f32_e32 v195, v195\n"
    "v_exp_f32_e64 v196, v196\n"
    "v_smfmac_f32_16x16x64_bf16 v[220:223], v[238:241], v[176:183], v235\n"
    "v_exp_f32_e64 v197, v197\n"
    "v_mfma_f32_32x32x16_bf16 v[144:159], a[168:171], a[88:91], v[144:159]\n"
    "v_exp_f32_e32 v198, v198\n"
    "v_exp_f32_e32 v199, v199\n"
    "v_exp_f32_e64 v200, v200\n"
    "v_mfma_f32_32x32x16_bf16 v[144:159], a[172:175], a[92:95], v[144:159]\n"
    "s_add_u32 m0, s52, 0\n"
    "v_exp_f32_e32 v201, v201\n"
    "buffer_load_dwordx4 v252, s[92:95], 0 offen offset:0 nt lds\n"
    "v_exp_f32_e32 v202, v202\n"
    "v_exp_f32_e32 v203, v203\n"
    "v_mfma_f32_32x32x16_bf16 v[96:111], v[16:19], v[208:211], 0\n"
    "v_exp_f32_e32 v204, v204\n"
    "v_exp_f32_e32 v205, v205\n"
    "v_cndmask_b32_e64 v204, 0, v204, s[54:55]\n"
    "v_cndmask_b32_e64 v205, 0, v205, s[54:55]\n"
    "v_mfma_f32_32x32x16_bf16 v[96:111], v[20:23], v[212:215], v[96:111]\n"
    "v_add_f32_e32 v206, v204, v205\n"
    "v_mul_f32_e32 v206, 0.5, v206\n"
    "v_add_f32_e32 v206, v192, v206\n"
    "v_add_f32_e32 v206, v193, v206\n"
    "v_add_f32_e32 v206, v194, v206\n"
    "v_add_f32_e32 v206, v195, v206\n"
    "v_mfma_f32_32x32x16_bf16 v[176:191], a[16:19], a[128:131], 0\n"
    "v_add_f32_e32 v206, v196, v206\n"
    "v_add_f32_e32 v206, v197, v206\n"
    "v_add_f32_e32 v206, v198, v206\n"
    "v_add_f32_e32 v206, v199, v206\n"
    "v_add_f32_e32 v206, v200, v206\n"
    "v_add_f32_e32 v206, v201, v206\n"
    "v_mfma_f32_32x32x16_bf16 v[176:191], a[20:23], a[132:135], v[176:191]\n"
    "v_add_f32_e32 v206, v202, v206\n"
    "v_add_f32_e32 v206, v203, v206\n"
    "v_cvt_pk_bf16_f32 v192, v192, v193\n"
    "v_cvt_pk_bf16_f32 v193, v194, v195\n"
    "v_cvt_pk_bf16_f32 v196, v196, v197\n"
    "v_cvt_pk_bf16_f32 v197, v198, v199\n"
    "v_mfma_f32_32x32x16_bf16 v[176:191], a[24:27], a[136:139], v[176:191]\n"
    "ds_write_b64 v250, v[192:193] offset:0\n"
    "v_cvt_pk_bf16_f32 v200, v200, v201\n"
    "v_cvt_pk_bf16_f32 v201, v202, v203\n"
    "v_mfma_f32_32x32x16_bf16 v[176:191], a[28:31], a[140:143], v[176:191]\n"
    "ds_write_b64 v250, v[196:197] offset:576\n"
    "buffer_load_dwordx4 v253, s[92:95], 0 offen offset:1024 nt lds\n"
    "v_cvt_pk_bf16_f32 v204, v204, v205\n"
    "v_add_f32_dpp v206, v206, v206 quad_perm:[1,0,3,2] row_mask:0xf bank_mask:0xf\n"
    "v_mov_b32_e32 v207, v206\n"
    "v_mov_b32_e32 v203, 0\n"
    "v_cndmask_b32_e64 v202, v204, 0, s[56:57]\n"
    "v_permlane16_swap_b32_e32 v206, v207\n"
    "v_add_f32_e32 v206, v206, v207\n"
    "v_mov_b32_e64 v207, v206\n"
    "v_cvt_pk_bf16_f32 v88, v144, v145\n"
    "v_cvt_pk_bf16_f32 v89, v146, v147\n"
    "v_permlane32_swap_b32_e32 v206, v207\n"
    "v_add_f32_e32 v207, v206, v207\n"
    "v_rcp_f32_e64 v206, v207\n"
    "v_cvt_pk_bf16_f32 v90, v148, v149\n"
    "v_maximum3_f32 v233, v233, v207, v206\n"
    "v_cvt_pk_bf16_f32 v91, v150, v151\n"
    "v_cvt_pk_bf16_f32 v92, v152, v153\n"
    "v_cvt_pk_bf16_f32 v93, v154, v155\n"
    "v_cvt_pk_bf16_f32 v94, v156, v157\n"
    "v_cvt_pk_bf16_f32 v95, v158, v159\n"
    "v_exp_f32_e32 v128, v128\n"
    "v_exp_f32_e32 v129, v129\n"
    "v_exp_f32_e32 v130, v130\n"
    "v_exp_f32_e32 v131, v131\n"
    "v_exp_f32_e32 v132, v132\n"
    "v_exp_f32_e32 v133, v133\n"
    "v_exp_f32_e32 v134, v134\n"
    "v_exp_f32_e32 v135, v135\n"
    "v_cvt_pk_bf16_f32 v128, v128, v129\n"
    "v_cvt_pk_bf16_f32 v129, v130, v131\n"
    "ds_write_b64 v250, v[200:201] offset:1152\n"
    "v_cvt_pk_bf16_f32 v130, v132, v133\n"
    "v_cvt_pk_bf16_f32 v131, v134, v135\n"
    "v_mov_b32_e64 v201, 0\n"
    "v_cndmask_b32_e64 v200, 0, v204, s[56:57]\n"
    "v_mfma_f32_32x32x16_bf16 v[112:127], v[56:59], v[128:131], v[112:127]\n"
    "buffer_load_dwordx4 v252, s[92:95], 0 offen offset:2048 nt lds\n"
    "v_exp_f32_e32 v136, v136\n"
    "v_exp_f32_e32 v137, v137\n"
    "v_exp_f32_e64 v138, v138\n"
    "v_mfma_f32_16x16x16_bf16 v[192:195], v[224:225], v[200:201], 0\n"
    "v_exp_f32_e32 v139, v139\n"
    "v_exp_f32_e32 v140, v140\n"
    "v_mfma_f32_16x16x16_bf16 v[192:195], v[226:227], v[202:203], v[192:195]\n"
    "v_exp_f32_e32 v141, v141\n"
    "v_exp_f32_e32 v142, v142\n"
    "v_exp_f32_e64 v143, v143\n"
    "v_cvt_pk_bf16_f32 v132, v136, v137\n"
    "v_cvt_pk_bf16_f32 v133, v138, v139\n"
    "v_cvt_pk_bf16_f32 v134, v140, v141\n"
    "v_cvt_pk_bf16_f32 v135, v142, v143\n"
    "v_exp_f32_e32 v96, v96\n"
    "v_exp_f32_e32 v97, v97\n"
    "v_mfma_f32_32x32x16_bf16 v[112:127], v[60:63], v[132:135], v[112:127]\n"
    "ds_read_b64 v[198:199], v250 offset:1728\n"
    "v_exp_f32_e32 v98, v98\n"
    "v_exp_f32_e32 v99, v99\n"
    "v_exp_f32_e64 v100, v100\n"
    "v_smfmac_f32_16x16x64_bf16 v[220:223], v[238:241], v[128:135], v235\n"
    "ds_read_b128 a[240:243], v247 offset:0\n"
    "v_exp_f32_e64 v101, v101\n"
    "v_mfma_f32_32x32x16_bf16 v[128:143], a[112:115], a[128:131], 0\n"
    "ds_read_b128 a[244:247], v247 offset:32\n"
    "v_exp_f32_e32 v102, v102\n"
    "v_exp_f32_e32 v103, v103\n"
    "v_cvt_pk_bf16_f32 v96, v96, v97\n"
    "v_mfma_f32_32x32x16_bf16 v[128:143], a[116:119], a[132:135], v[128:143]\n"
    "ds_read_b128 a[248:251], v247 offset:576\n"
    "buffer_load_dwordx4 v253, s[92:95], 0 offen offset:3072 nt lds\n"
    "v_cvt_pk_bf16_f32 v97, v98, v99\n"
    "v_cvt_pk_bf16_f32 v98, v100, v101\n"
    "v_cvt_pk_bf16_f32 v99, v102, v103\n"
    "v_cvt_pk_bf16_f32 v208, v176, v177\n"
    "v_mfma_f32_32x32x16_bf16 v[128:143], a[120:123], a[136:139], v[128:143]\n"
    "ds_read_b128 a[224:227], v247 offset:608\n"
    "v_cvt_pk_bf16_f32 v209, v178, v179\n"
    "v_cvt_pk_bf16_f32 v210, v180, v181\n"
    "v_cvt_pk_bf16_f32 v211, v182, v183\n"
    "v_cvt_pk_bf16_f32 v212, v184, v185\n"
    "v_cvt_pk_bf16_f32 v213, v186, v187\n"
    "v_mfma_f32_32x32x16_bf16 v[112:127], v[64:67], v[96:99], v[112:127]\n"
    "ds_read_b128 a[228:231], v247 offset:1152\n"
    "v_cvt_pk_bf16_f32 v214, v188, v189\n"
    "v_cvt_pk_bf16_f32 v215, v190, v191\n"
    "v_exp_f32_e32 v104, v104\n"
    "v_exp_f32_e32 v105, v105\n"
    "v_mfma_f32_32x32x16_bf16 v[144:159], v[24:27], v[208:211], 0\n"
    "ds_read_b128 a[232:235], v247 offset:1184\n"
    "v_exp_f32_e32 v106, v106\n"
    "v_exp_f32_e32 v107, v107\n"
    "v_exp_f32_e64 v108, v108\n"
    "v_mfma_f32_32x32x16_bf16 v[144:159], v[28:31], v[212:215], v[144:159]\n"
    "s_waitcnt lgkmcnt(4)\n"
    "ds_read_b128 a[236:239], v248 offset:64\n"
    "v_exp_f32_e32 v109, v109\n"
    "v_exp_f32_e64 v110, v110\n"
    "v_mfma_f32_16x16x32_bf16 v[216:219], a[184:187], v[208:211], 0\n"
    "s_waitcnt vmcnt(12)\n"
    "v_exp_f32_e32 v111, v111\n"
    "v_mfma_f32_16x16x32_bf16 v[216:219], a[188:191], v[212:215], v[216:219]\n"
    "ds_read_b128 a[128:131], v243 offset:0\n"
    "v_cvt_pk_bf16_f32 v100, v104, v105\n"
    "v_mfma_f32_32x32x16_bf16 v[176:191], v[32:35], v[208:211], 0\n"
    "ds_read_b128 a[132:135], v244 offset:0\n"
    "v_cvt_pk_bf16_f32 v101, v106, v107\n"
    "v_cvt_pk_bf16_f32 v102, v108, v109\n"
    "v_cvt_pk_bf16_f32 v103, v110, v111\n"
    "v_lshlrev_b32_e32 v196, 16, v198\n"
    "v_and_b32_e32 v197, s61, v198\n"
    "v_mfma_f32_32x32x16_bf16 v[112:127], v[68:71], v[100:103], v[112:127]\n"
    "s_add_u32 m0, s52, s96\n"
    "v_lshlrev_b32_e32 v198, 16, v199\n"
    "buffer_load_dwordx4 v252, s[92:95], s96 offen offset:0 nt lds\n"
    "v_and_b32_e32 v199, s61, v199\n"
    "v_exp_f32_e32 v216, v216\n"
    "v_exp_f32_e64 v217, v217\n"
    "v_smfmac_f32_16x16x64_bf16 v[220:223], v[238:241], v[96:103], v235\n"
    "ds_read_b128 a[136:139], v245 offset:0\n"
    "v_exp_f32_e64 v144, v144\n"
    "v_mfma_f32_32x32x16_bf16 v[176:191], v[36:39], v[212:215], v[176:191]\n"
    "s_waitcnt lgkmcnt(3)\n"
    "v_exp_f32_e32 v145, v145\n"
    "v_exp_f32_e32 v146, v146\n"
    "v_exp_f32_e32 v147, v147\n"
    "v_mfma_f32_16x16x32_bf16 v[192:195], v[48:51], a[240:243], v[192:195]\n"
    "ds_read_b128 a[240:243], v248 offset:96\n"
    "v_exp_f32_e64 v148, v148\n"
    "v_mfma_f32_16x16x32_bf16 v[192:195], v[52:55], a[244:247], v[192:195]\n"
    "ds_read_b128 a[244:247], v248 offset:640\n"
    "v_exp_f32_e64 v149, v149\n"
    "v_mfma_f32_16x16x32_bf16 v[192:195], v[56:59], a[248:251], v[192:195]\n"
    "ds_read_b128 a[248:251], v248 offset:672\n"
    "v_exp_f32_e64 v150, v150\n"
    "v_mfma_f32_16x16x32_bf16 v[192:195], v[60:63], a[224:227], v[192:195]\n"
    "ds_read_b128 a[224:227], v248 offset:1216\n"
    "v_exp_f32_e64 v151, v151\n"
    "v_mfma_f32_16x16x32_bf16 v[192:195], v[64:67], a[228:231], v[192:195]\n"
    "ds_read_b128 a[228:231], v248 offset:1248\n"
    "v_cvt_pk_bf16_f32 v144, v144, v145\n"
    "v_mfma_f32_16x16x32_bf16 v[192:195], v[68:71], a[232:235], v[192:195]\n"
    "s_waitcnt lgkmcnt(5)\n"
    "v_cvt_pk_bf16_f32 v145, v146, v147\n"
    "v_cvt_pk_bf16_f32 v146, v148, v149\n"
    "v_mfma_f32_16x16x32_bf16 v[192:195], v[72:75], a[236:239], v[192:195]\n"
    "v_cvt_pk_bf16_f32 v147, v150, v151\n"
    "buffer_load_dwordx4 v253, s[92:95], s96 offen offset:1024 nt lds\n"
    "v_mfma_f32_32x32x16_bf16 v[128:143], a[124:127], a[140:143], v[128:143]\n"
    "ds_read_b128 a[140:143], v246 offset:0\n"
    "v_add_f32_e32 v236, v220, v221\n"
    "v_rcp_f32_e64 v232, v236\n"
    "v_cvt_pk_bf16_f32 v230, v216, v217\n"
    "v_fmac_f32_e32 v160, v112, v232\n"
    "s_waitcnt lgkmcnt(5)\n"
    "v_mfma_f32_16x16x32_bf16 v[192:195], v[76:79], a[240:243], v[192:195]\n"
    "s_waitcnt lgkmcnt(4)\n"
    "v_fmac_f32_e32 v161, v113, v232\n"
    "v_fmac_f32_e64 v162, v114, v232\n"
    "v_mfma_f32_16x16x32_bf16 v[192:195], v[80:83], a[244:247], v[192:195]\n"
    "v_fmac_f32_e32 v163, v115, v232\n"
    "v_fmac_f32_e64 v164, v116, v232\n"
    "s_waitcnt lgkmcnt(3)\n"
    "v_mfma_f32_16x16x32_bf16 v[192:195], v[84:87], a[248:251], v[192:195]\n"
    "v_fmac_f32_e32 v165, v117, v232\n"
    "v_fmac_f32_e64 v166, v118, v232\n"
    "s_waitcnt lgkmcnt(2)\n"
    "v_mfma_f32_16x16x32_bf16 v[192:195], v[88:91], a[224:227], v[192:195]\n"
    "v_fmac_f32_e32 v167, v119, v232\n"
    "v_fmac_f32_e64 v168, v120, v232\n"
    "s_waitcnt lgkmcnt(1)\n"
    "v_mfma_f32_16x16x32_bf16 v[192:195], v[92:95], a[228:231], v[192:195]\n"
    "v_fmac_f32_e32 v169, v121, v232\n"
    "v_fmac_f32_e32 v170, v122, v232\n"
    "v_mfma_f32_16x16x16_bf16 v[220:223], v[234:235], v[230:231], 0\n"
    "v_fmac_f32_e32 v171, v123, v232\n"
    "v_fmac_f32_e32 v172, v124, v232\n"
    "v_fmac_f32_e32 v173, v125, v232\n"
    "v_fmac_f32_e32 v174, v126, v232\n"
    "v_fmac_f32_e32 v196, v192, v206\n"
    "v_fmac_f32_e32 v197, v193, v206\n"
    "v_fmac_f32_e32 v198, v194, v206\n"
    "v_fmac_f32_e32 v199, v195, v206\n"
    "v_cvt_pk_bf16_f32 v196, v196, v197\n"
    "v_cvt_pk_bf16_f32 v197, v198, v199\n"
    "v_pk_max_i16 v196, v196, 0\n"
    "v_pk_max_i16 v197, v197, 0\n"
    "v_fmac_f32_e64 v175, v127, v232\n"
    "v_mfma_f32_32x32x8_bf16 v[112:127], v[226:227], v[230:231], 0\n"
    "v_maximum3_f32 v233, v233, v236, v232\n"
    "v_cvt_pk_bf16_f32 v160, v160, v161\n"
    "v_cvt_pk_bf16_f32 v161, v162, v163\n"
    "v_cvt_pk_bf16_f32 v162, v164, v165\n"
    "buffer_load_dwordx4 v252, s[92:95], s96 offen offset:2048 nt lds\n"
    "v_cvt_pk_bf16_f32 v163, v166, v167\n"
    "buffer_store_dwordx2 v[196:197], v237, s[44:47], s98 offen nt\n"
    "v_pk_max_i16 v160, v160, 0\n"
    "v_pk_max_i16 v161, v161, 0\n"
    "v_mfma_f32_32x32x16_bf16 v[192:207], v[40:43], v[208:211], 0\n"
    "s_waitcnt lgkmcnt(0)\n"
    "v_pk_max_i16 v162, v162, 0\n"
    "v_pk_max_i16 v163, v163, 0\n"
    "v_cvt_pk_bf16_f32 v168, v168, v169\n"
    "v_cvt_pk_bf16_f32 v169, v170, v171\n"
    "v_cvt_pk_bf16_f32 v170, v172, v173\n"
    "v_cvt_pk_bf16_f32 v171, v174, v175\n"
    "v_mfma_f32_32x32x16_bf16 v[112:127], v[72:75], v[144:147], v[112:127]\n"
    "ds_write_b128 v251, v[160:163] offset:27648\n"
    "v_pk_max_i16 v168, v168, 0\n"
    "v_pk_max_i16 v169, v169, 0\n"
    "v_mfma_f32_32x32x16_bf16 v[192:207], v[44:47], v[212:215], v[192:207]\n"
    "v_pk_max_i16 v170, v170, 0\n"
    "v_pk_max_i16 v171, v171, 0\n"
    "v_exp_f32_e32 v152, v152\n"
    "v_exp_f32_e32 v153, v153\n"
    "v_exp_f32_e32 v154, v154\n"
    "v_exp_f32_e32 v155, v155\n"
    "v_exp_f32_e32 v156, v156\n"
    "v_exp_f32_e32 v157, v157\n"
    "v_exp_f32_e32 v158, v158\n"
    "v_exp_f32_e64 v159, v159\n"
    "v_cvt_pk_bf16_f32 v148, v152, v153\n"
    "v_cvt_pk_bf16_f32 v149, v154, v155\n"
    "v_cvt_pk_bf16_f32 v150, v156, v157\n"
    "v_cvt_pk_bf16_f32 v151, v158, v159\n"
    "v_exp_f32_e32 v176, v176\n"
    "v_exp_f32_e32 v177, v177\n"
    "v_mfma_f32_32x32x16_bf16 v[112:127], v[76:79], v[148:151], v[112:127]\n"
    "buffer_load_dwordx4 v253, s[92:95], s96 offen offset:3072 nt lds\n"
    "v_exp_f32_e32 v178, v178\n"
    "v_exp_f32_e32 v179, v179\n"
    "v_exp_f32_e64 v180, v180\n"
    "v_smfmac_f32_16x16x64_bf16 v[220:223], v[238:241], v[144:151], v235\n"
    "ds_write_b128 v251, v[168:171] offset:28672\n"
    "v_exp_f32_e32 v181, v181\n"
    "v_exp_f32_e32 v182, v182\n"
    "v_mfma_f32_32x32x16_bf16 v[160:175], a[16:19], a[144:147], 0\n"
    "s_add_u32 m0, s35, 0\n"
    "v_exp_f32_e32 v183, v183\n"
    "v_cvt_pk_bf16_f32 v176, v176, v177\n"
    "v_cvt_pk_bf16_f32 v177, v178, v179\n"
    "v_cvt_pk_bf16_f32 v178, v180, v181\n"
    "v_cvt_pk_bf16_f32 v179, v182, v183\n"
    "v_mfma_f32_32x32x16_bf16 v[160:175], a[20:23], a[148:151], v[160:175]\n"
    "v_exp_f32_e32 v184, v184\n"
    "v_exp_f32_e32 v185, v185\n"
    "v_exp_f32_e64 v186, v186\n"
    "v_mfma_f32_32x32x16_bf16 v[112:127], v[80:83], v[176:179], v[112:127]\n"
    "v_exp_f32_e32 v187, v187\n"
    "v_exp_f32_e32 v188, v188\n"
    "v_exp_f32_e64 v189, v189\n"
    "v_mfma_f32_32x32x16_bf16 v[160:175], a[24:27], a[152:155], v[160:175]\n"
    "v_exp_f32_e32 v190, v190\n"
    "v_exp_f32_e32 v191, v191\n"
    "v_cvt_pk_bf16_f32 v180, v184, v185\n"
    "v_cvt_pk_bf16_f32 v181, v186, v187\n"
    "v_mfma_f32_32x32x16_bf16 v[160:175], a[28:31], a[156:159], v[160:175]\n"
    "v_cvt_pk_bf16_f32 v182, v188, v189\n"
    "v_cvt_pk_bf16_f32 v183, v190, v191\n"
    "v_exp_f32_e32 v192, v192\n"
    "v_exp_f32_e32 v193, v193\n"
    "v_mfma_f32_32x32x16_bf16 v[112:127], v[84:87], v[180:183], v[112:127]\n"
    "buffer_load_dwordx4 v252, s[48:51], 0 offen offset:0 nt lds\n"
    "v_exp_f32_e32 v194, v194\n"
    "v_exp_f32_e32 v195, v195\n"
    "v_exp_f32_e64 v196, v196\n"
    "v_smfmac_f32_16x16x64_bf16 v[220:223], v[238:241], v[176:183], v235\n"
    "v_exp_f32_e32 v197, v197\n"
    "v_exp_f32_e32 v198, v198\n"
    "v_exp_f32_e64 v199, v199\n"
    "v_cvt_pk_bf16_f32 v192, v192, v193\n"
    "v_cvt_pk_bf16_f32 v193, v194, v195\n"
    "v_cvt_pk_bf16_f32 v194, v196, v197\n"
    "v_cvt_pk_bf16_f32 v195, v198, v199\n"
    "v_cvt_pk_bf16_f32 v208, v160, v161\n"
    "v_cvt_pk_bf16_f32 v209, v162, v163\n"
    "v_mfma_f32_32x32x16_bf16 v[112:127], v[88:91], v[192:195], v[112:127]\n"
    "v_cvt_pk_bf16_f32 v210, v164, v165\n"
    "v_cvt_pk_bf16_f32 v211, v166, v167\n"
    "v_cvt_pk_bf16_f32 v212, v168, v169\n"
    "v_cvt_pk_bf16_f32 v213, v170, v171\n"
    "v_cvt_pk_bf16_f32 v214, v172, v173\n"
    "v_cvt_pk_bf16_f32 v215, v174, v175\n"
    "v_mfma_f32_32x32x16_bf16 v[96:111], v[24:27], v[208:211], 0\n"
    "v_exp_f32_e32 v200, v200\n"
    "v_exp_f32_e32 v201, v201\n"
    "v_exp_f32_e64 v202, v202\n"
    "v_mfma_f32_32x32x16_bf16 v[96:111], v[28:31], v[212:215], v[96:111]\n"
    "v_exp_f32_e32 v203, v203\n"
    "v_exp_f32_e32 v204, v204\n"
    "v_exp_f32_e64 v205, v205\n"
    "v_mfma_f32_16x16x32_bf16 v[216:219], a[184:187], v[208:211], 0\n"
    "v_exp_f32_e64 v206, v206\n"
    "v_mfma_f32_16x16x32_bf16 v[216:219], a[188:191], v[212:215], v[216:219]\n"
    "v_exp_f32_e64 v207, v207\n"
    "v_cvt_pk_bf16_f32 v196, v200, v201\n"
    "v_cvt_pk_bf16_f32 v197, v202, v203\n"
    "v_cvt_pk_bf16_f32 v198, v204, v205\n"
    "v_cvt_pk_bf16_f32 v199, v206, v207\n"
    "buffer_load_dwordx4 v253, s[48:51], 0 offen offset:1024 nt lds\n"
    "buffer_load_dwordx4 v252, s[48:51], 0 offen offset:2048 nt lds\n"
    "v_mfma_f32_32x32x16_bf16 v[112:127], v[92:95], v[196:199], v[112:127]\n"
    "buffer_load_dwordx4 v253, s[48:51], 0 offen offset:3072 nt lds\n"
    "v_smfmac_f32_16x16x64_bf16 v[220:223], v[238:241], v[192:199], v235\n"
    "s_waitcnt lgkmcnt(0)\n"
    "s_cmp_lt_i32 s59, s36\n"
    "s_cbranch_scc0 .Llast_%=\n"
    ".p2align 6\n"
    ".Lloop1_%=:\n"
    "s_mov_b64 s[88:89], s[92:93]\n"
    "s_mov_b32 s90, s94\n"
    "s_add_u32 s92, s92, s39\n"
    "s_addc_u32 s93, s93, 0\n"
    "s_add_i32 s37, s37, s38\n"
    "s_cmp_lt_i32 s37, s36\n"
    "s_cselect_b32 s94, s60, 0\n"
    "s_add_i32 s59, s59, s38\n"
    "s_mov_b32 s34, s86\n"
    "s_mov_b32 s86, s87\n"
    "s_mov_b32 s87, s34\n"
    "s_add_u32 s34, s52, s87\n"
    "v_mfma_f32_32x32x16_bf16 v[144:159], v[32:35], v[208:211], 0\n"
    "v_add_u32_e32 v249, s58, v249\n"
    "v_add_u32_e32 v229, s58, v229\n"
    "v_add_u32_e32 v247, s58, v247\n"
    "v_add_u32_e32 v248, s58, v248\n"
    "v_add_u32_e32 v250, s58, v250\n"
    "v_exp_f32_e32 v216, v216\n"
    "v_mfma_f32_32x32x16_bf16 v[144:159], v[36:39], v[212:215], v[144:159]\n"
    "ds_read_b128 a[176:179], v249 offset:0\n"
    "v_exp_f32_e32 v217, v217\n"
    "v_exp_f32_e32 v96, v96\n"
    "v_exp_f32_e64 v97, v97\n"
    "v_mfma_f32_32x32x16_bf16 v[160:175], a[32:35], a[128:131], 0\n"
    "ds_read_b128 a[180:183], v249 offset:32\n"
    "v_exp_f32_e32 v98, v98\n"
    "v_exp_f32_e32 v99, v99\n"
    "v_exp_f32_e64 v100, v100\n"
    "v_mfma_f32_32x32x16_bf16 v[160:175], a[36:39], a[132:135], v[160:175]\n"
    "ds_read_b64 v[224:225], v229 offset:1152\n"
    "v_exp_f32_e32 v101, v101\n"
    "v_exp_f32_e32 v102, v102\n"
    "v_exp_f32_e64 v103, v103\n"
    "v_mfma_f32_32x32x16_bf16 v[160:175], a[40:43], a[136:139], v[160:175]\n"
    "ds_read_b128 a[192:195], v247 offset:576\n"
    "v_cvt_pk_bf16_f32 v96, v96, v97\n"
    "v_cvt_pk_bf16_f32 v97, v98, v99\n"
    "v_cvt_pk_bf16_f32 v98, v100, v101\n"
    "v_cvt_pk_bf16_f32 v99, v102, v103\n"
    "v_add_f32_e64 v236, v220, v221\n"
    "v_mfma_f32_32x32x16_bf16 v[160:175], a[44:47], a[140:143], v[160:175]\n"
    "ds_read_b128 a[196:199], v247 offset:608\n"
    "v_rcp_f32_e64 v232, v236\n"
    "v_cvt_pk_bf16_f32 v230, v216, v217\n"
    "v_fmac_f32_e32 v128, v112, v232\n"
    "v_fmac_f32_e32 v129, v113, v232\n"
    "v_mfma_f32_16x16x16_bf16 v[220:223], v[234:235], v[230:231], 0\n"
    "v_fmac_f32_e32 v130, v114, v232\n"
    "v_fmac_f32_e32 v131, v115, v232\n"
    "v_fmac_f32_e64 v132, v116, v232\n"
    "v_mfma_f32_32x32x8_bf16 v[176:191], v[226:227], v[230:231], 0\n"
    "v_cvt_pk_bf16_f32 v0, v160, v161\n"
    "v_cvt_pk_bf16_f32 v1, v162, v163\n"
    "v_cvt_pk_bf16_f32 v2, v164, v165\n"
    "v_cvt_pk_bf16_f32 v3, v166, v167\n"
    "v_cvt_pk_bf16_f32 v4, v168, v169\n"
    "v_cvt_pk_bf16_f32 v5, v170, v171\n"
    "v_cvt_pk_bf16_f32 v6, v172, v173\n"
    "v_mfma_f32_32x32x16_bf16 v[192:207], a[112:115], a[144:147], 0\n"
    "s_waitcnt vmcnt(17)\n"
    "v_cvt_pk_bf16_f32 v7, v174, v175\n"
    "v_fmac_f32_e32 v133, v117, v232\n"
    "v_fmac_f32_e32 v134, v118, v232\n"
    "v_fmac_f32_e32 v135, v119, v232\n"
    "v_fmac_f32_e32 v136, v120, v232\n"
    "v_fmac_f32_e32 v137, v121, v232\n"
    "v_mfma_f32_32x32x16_bf16 v[176:191], v[72:75], v[96:99], v[176:191]\n"
    "ds_read_b128 a[144:147], v243 offset:4096\n"
    "v_fmac_f32_e32 v138, v122, v232\n"
    "v_fmac_f32_e32 v139, v123, v232\n"
    "v_fmac_f32_e32 v140, v124, v232\n"
    "v_fmac_f32_e32 v141, v125, v232\n"
    "v_fmac_f32_e64 v142, v126, v232\n"
    "v_mfma_f32_32x32x16_bf16 v[192:207], a[116:119], a[148:151], v[192:207]\n"
    "ds_read_b128 a[148:151], v244 offset:4096\n"
    "v_fmac_f32_e64 v143, v127, v232\n"
    "v_maximum3_f32 v233, v233, v236, v232\n"
    "v_cvt_pk_bf16_f32 v128, v128, v129\n"
    "v_cvt_pk_bf16_f32 v129, v130, v131\n"
    "v_cvt_pk_bf16_f32 v130, v132, v133\n"
    "v_mfma_f32_32x32x16_bf16 v[192:207], a[120:123], a[152:155], v[192:207]\n"
    "ds_read_b128 a[152:155], v245 offset:4096\n"
    "v_cvt_pk_bf16_f32 v131, v134, v135\n"
    "v_pk_max_i16 v128, v128, 0\n"
    "v_pk_max_i16 v129, v129, 0\n"
    "v_pk_max_i16 v130, v130, 0\n"
    "v_pk_max_i16 v131, v131, 0\n"
    "v_mfma_f32_32x32x16_bf16 v[192:207], a[124:127], a[156:159], v[192:207]\n"
    "ds_write_b128 v251, v[128:131] offset:21504\n"
    "v_cvt_pk_bf16_f32 v136, v136, v137\n"
    "v_cvt_pk_bf16_f32 v137, v138, v139\n"
    "v_mfma_f32_32x32x16_bf16 v[160:175], a[128:131], a[64:67], 0\n"
    "ds_read_b128 a[156:159], v246 offset:4096\n"
    "v_cvt_pk_bf16_f32 v138, v140, v141\n"
    "v_cvt_pk_bf16_f32 v139, v142, v143\n"
    "v_pk_max_i16 v136, v136, 0\n"
    "v_pk_max_i16 v137, v137, 0\n"
    "v_pk_max_i16 v138, v138, 0\n"
    "v_mfma_f32_32x32x16_bf16 v[160:175], a[132:135], a[68:71], v[160:175]\n"
    "ds_read_b128 a[200:203], v248 offset:640\n"
    "v_pk_max_i16 v139, v139, 0\n"
    "v_exp_f32_e32 v104, v104\n"
    "v_exp_f32_e32 v105, v105\n"
    "v_mfma_f32_32x32x16_bf16 v[160:175], a[136:139], a[72:75], v[160:175]\n"
    "ds_write_b128 v251, v[136:139] offset:22528\n"
    "v_exp_f32_e64 v106, v106\n"
    "v_mfma_f32_32x32x16_bf16 v[160:175], a[140:143], a[76:79], v[160:175]\n"
    "ds_read_b128 a[208:211], v254 offset:21504\n"
    "v_exp_f32_e32 v107, v107\n"
    "v_exp_f32_e32 v108, v108\n"
    "v_exp_f32_e64 v109, v109\n"
    "v_mfma_f32_32x32x16_bf16 v[112:127], v[40:43], v[208:211], 0\n"
    "ds_read_b128 a[204:207], v248 offset:672\n"
    "v_exp_f32_e32 v110, v110\n"
    "v_exp_f32_e32 v111, v111\n"
    "v_cvt_pk_bf16_f32 v100, v104, v105\n"
    "v_mfma_f32_32x32x16_bf16 v[112:127], v[44:47], v[212:215], v[112:127]\n"
    "s_waitcnt lgkmcnt(4)\n"
    "s_add_u32 m0, s34, s96\n"
    "v_cvt_pk_bf16_f32 v101, v106, v107\n"
    "v_cvt_pk_bf16_f32 v102, v108, v109\n"
    "v_cvt_pk_bf16_f32 v103, v110, v111\n"
    "v_cvt_pk_bf16_f32 v48, v160, v161\n"
    "v_cvt_pk_bf16_f32 v49, v162, v163\n"
    "v_mfma_f32_32x32x16_bf16 v[176:191], v[76:79], v[100:103], v[176:191]\n"
    "ds_read_b128 a[212:215], v254 offset:21632\n"
    "v_cvt_pk_bf16_f32 v50, v164, v165\n"
    "v_cvt_pk_bf16_f32 v51, v166, v167\n"
    "v_cvt_pk_bf16_f32 v52, v168, v169\n"
    "v_cvt_pk_bf16_f32 v53, v170, v171\n"
    "v_cvt_pk_bf16_f32 v54, v172, v173\n"
    "v_smfmac_f32_16x16x64_bf16 v[220:223], v[238:241], v[96:103], v235\n"
    "s_waitcnt lgkmcnt(3)\n"
    "v_cvt_pk_bf16_f32 v55, v174, v175\n"
    "v_mfma_f32_32x32x16_bf16 v[128:143], a[16:19], a[160:163], 0\n"
    "s_waitcnt lgkmcnt(2)\n"
    "v_exp_f32_e32 v144, v144\n"
    "v_exp_f32_e32 v145, v145\n"
    "v_exp_f32_e64 v146, v146\n"
    "v_mfma_f32_32x32x16_bf16 v[128:143], a[20:23], a[164:167], v[128:143]\n"
    "s_waitcnt lgkmcnt(1)\n"
    "v_exp_f32_e32 v147, v147\n"
    "v_exp_f32_e32 v148, v148\n"
    "v_exp_f32_e32 v149, v149\n"
    "v_mfma_f32_32x32x16_bf16 v[128:143], a[24:27], a[168:171], v[128:143]\n"
    "v_exp_f32_e32 v150, v150\n"
    "v_exp_f32_e32 v151, v151\n"
    "v_cvt_pk_bf16_f32 v144, v144, v145\n"
    "v_cvt_pk_bf16_f32 v145, v146, v147\n"
    "v_mfma_f32_32x32x16_bf16 v[128:143], a[28:31], a[172:175], v[128:143]\n"
    "s_waitcnt lgkmcnt(0)\n"
    "v_cvt_pk_bf16_f32 v146, v148, v149\n"
    "v_cvt_pk_bf16_f32 v147, v150, v151\n"
    "v_exp_f32_e32 v152, v152\n"
    "v_exp_f32_e64 v153, v153\n"
    "v_mfma_f32_32x32x16_bf16 v[176:191], v[80:83], v[144:147], v[176:191]\n"
    "v_exp_f32_e32 v154, v154\n"
    "v_exp_f32_e32 v155, v155\n"
    "v_exp_f32_e32 v156, v156\n"
    "v_exp_f32_e32 v157, v157\n"
    "v_exp_f32_e32 v158, v158\n"
    "v_exp_f32_e32 v159, v159\n"
    "v_cvt_pk_bf16_f32 v148, v152, v153\n"
    "v_cvt_pk_bf16_f32 v149, v154, v155\n"
    "v_cvt_pk_bf16_f32 v150, v156, v157\n"
    "v_cvt_pk_bf16_f32 v151, v158, v159\n"
    "v_exp_f32_e32 v112, v112\n"
    "v_exp_f32_e32 v113, v113\n"
    "v_mfma_f32_32x32x16_bf16 v[176:191], v[84:87], v[148:151], v[176:191]\n"
    "buffer_load_dwordx4 v252, s[92:95], s96 offen offset:0 nt lds\n"
    "v_exp_f32_e32 v114, v114\n"
    "v_exp_f32_e32 v115, v115\n"
    "v_exp_f32_e64 v116, v116\n"
    "v_smfmac_f32_16x16x64_bf16 v[220:223], v[238:241], v[144:151], v235\n"
    "ds_read_b128 a[216:219], v254 offset:21760\n"
    "v_exp_f32_e64 v117, v117\n"
    "v_mfma_f32_32x32x16_bf16 v[144:159], a[32:35], a[144:147], 0\n"
    "v_exp_f32_e32 v118, v118\n"
    "v_exp_f32_e32 v119, v119\n"
    "v_cvt_pk_bf16_f32 v112, v112, v113\n"
    "v_cvt_pk_bf16_f32 v113, v114, v115\n"
    "v_mfma_f32_32x32x16_bf16 v[144:159], a[36:39], a[148:151], v[144:159]\n"
    "v_cvt_pk_bf16_f32 v114, v116, v117\n"
    "v_cvt_pk_bf16_f32 v115, v118, v119\n"
    "v_cvt_pk_bf16_f32 v208, v128, v129\n"
    "v_cvt_pk_bf16_f32 v209, v130, v131\n"
    "v_cvt_pk_bf16_f32 v210, v132, v133\n"
    "v_cvt_pk_bf16_f32 v211, v134, v135\n"
    "v_mfma_f32_32x32x16_bf16 v[176:191], v[88:91], v[112:115], v[176:191]\n"
    "v_cvt_pk_bf16_f32 v212, v136, v137\n"
    "v_cvt_pk_bf16_f32 v213, v138, v139\n"
    "v_cvt_pk_bf16_f32 v214, v140, v141\n"
    "v_cvt_pk_bf16_f32 v215, v142, v143\n"
    "v_exp_f32_e64 v120, v120\n"
    "v_mfma_f32_32x32x16_bf16 v[96:111], v[24:27], v[208:211], 0\n"
    "v_exp_f32_e32 v121, v121\n"
    "v_exp_f32_e32 v122, v122\n"
    "v_exp_f32_e64 v123, v123\n"
    "v_mfma_f32_32x32x16_bf16 v[96:111], v[28:31], v[212:215], v[96:111]\n"
    "s_waitcnt lgkmcnt(0)\n"
    "v_exp_f32_e32 v124, v124\n"
    "v_exp_f32_e32 v125, v125\n"
    "v_exp_f32_e32 v126, v126\n"
    "v_mfma_f32_16x16x32_bf16 v[216:219], a[184:187], v[208:211], 0\n"
    "ds_read_b128 a[184:187], v249 offset:64\n"
    "v_exp_f32_e64 v127, v127\n"
    "v_mfma_f32_16x16x32_bf16 v[216:219], a[188:191], v[212:215], v[216:219]\n"
    "ds_read_b128 a[188:191], v249 offset:96\n"
    "v_cvt_pk_bf16_f32 v116, v120, v121\n"
    "v_mfma_f32_32x32x16_bf16 v[160:175], v[32:35], v[208:211], 0\n"
    "v_cvt_pk_bf16_f32 v117, v122, v123\n"
    "v_cvt_pk_bf16_f32 v118, v124, v125\n"
    "v_cvt_pk_bf16_f32 v119, v126, v127\n"
    "v_exp_f32_e32 v96, v96\n"
    "v_exp_f32_e32 v216, v216\n"
    "v_mfma_f32_32x32x16_bf16 v[176:191], v[92:95], v[116:119], v[176:191]\n"
    "ds_read_b128 a[220:223], v254 offset:21888\n"
    "v_exp_f32_e32 v217, v217\n"
    "v_exp_f32_e32 v97, v97\n"
    "v_exp_f32_e64 v98, v98\n"
    "v_smfmac_f32_16x16x64_bf16 v[220:223], v[238:241], v[112:119], v235\n"
    "buffer_load_dwordx4 v253, s[92:95], s96 offen offset:1024 nt lds\n"
    "v_exp_f32_e64 v99, v99\n"
    "v_mfma_f32_32x32x16_bf16 v[160:175], v[36:39], v[212:215], v[160:175]\n"
    "v_exp_f32_e32 v100, v100\n"
    "v_exp_f32_e32 v101, v101\n"
    "v_exp_f32_e64 v102, v102\n"
    "v_mfma_f32_32x32x16_bf16 v[144:159], a[40:43], a[152:155], v[144:159]\n"
    "s_waitcnt lgkmcnt(2)\n"
    "v_exp_f32_e32 v103, v103\n"
    "v_cvt_pk_bf16_f32 v96, v96, v97\n"
    "v_cvt_pk_bf16_f32 v97, v98, v99\n"
    "v_cvt_pk_bf16_f32 v98, v100, v101\n"
    "v_cvt_pk_bf16_f32 v99, v102, v103\n"
    "v_mfma_f32_32x32x16_bf16 v[144:159], a[44:47], a[156:159], v[144:159]\n"
    "s_waitcnt lgkmcnt(1)\n"
    "v_add_f32_e32 v236, v220, v221\n"
    "v_rcp_f32_e64 v232, v236\n"
    "v_cvt_pk_bf16_f32 v230, v216, v217\n"
    "v_fmac_f32_e32 v192, v176, v232\n"
    "v_fmac_f32_e32 v193, v177, v232\n"
    "v_mfma_f32_16x16x16_bf16 v[220:223], v[234:235], v[230:231], 0\n"
    "v_fmac_f32_e32 v194, v178, v232\n"
    "v_fmac_f32_e32 v195, v179, v232\n"
    "v_fmac_f32_e64 v196, v180, v232\n"
    "v_mfma_f32_32x32x8_bf16 v[128:143], v[226:227], v[230:231], 0\n"
    "v_cvt_pk_bf16_f32 v8, v144, v145\n"
    "v_cvt_pk_bf16_f32 v9, v146, v147\n"
    "v_cvt_pk_bf16_f32 v10, v148, v149\n"
    "v_cvt_pk_bf16_f32 v11, v150, v151\n"
    "v_cvt_pk_bf16_f32 v12, v152, v153\n"
    "v_cvt_pk_bf16_f32 v13, v154, v155\n"
    "v_cvt_pk_bf16_f32 v14, v156, v157\n"
    "v_mfma_f32_32x32x16_bf16 v[112:127], a[112:115], a[160:163], 0\n"
    "s_waitcnt lgkmcnt(0)\n"
    "v_cvt_pk_bf16_f32 v15, v158, v159\n"
    "v_fmac_f32_e32 v197, v181, v232\n"
    "v_fmac_f32_e32 v198, v182, v232\n"
    "v_fmac_f32_e32 v199, v183, v232\n"
    "v_fmac_f32_e32 v200, v184, v232\n"
    "v_fmac_f32_e32 v201, v185, v232\n"
    "v_mfma_f32_32x32x16_bf16 v[128:143], v[72:75], v[96:99], v[128:143]\n"
    "v_fmac_f32_e32 v202, v186, v232\n"
    "v_fmac_f32_e32 v203, v187, v232\n"
    "v_fmac_f32_e32 v204, v188, v232\n"
    "v_fmac_f32_e32 v205, v189, v232\n"
    "v_fmac_f32_e32 v206, v190, v232\n"
    "v_fmac_f32_e32 v207, v191, v232\n"
    "v_mfma_f32_32x32x16_bf16 v[112:127], a[116:119], a[164:167], v[112:127]\n"
    "v_maximum3_f32 v233, v233, v236, v232\n"
    "v_cvt_pk_bf16_f32 v192, v192, v193\n"
    "v_cvt_pk_bf16_f32 v193, v194, v195\n"
    "v_cvt_pk_bf16_f32 v194, v196, v197\n"
    "v_cvt_pk_bf16_f32 v195, v198, v199\n"
    "v_pk_max_i16 v192, v192, 0\n"
    "v_mfma_f32_32x32x16_bf16 v[112:127], a[120:123], a[168:171], v[112:127]\n"
    "buffer_load_dwordx4 v252, s[92:95], s96 offen offset:2048 nt lds\n"
    "v_pk_max_i16 v193, v193, 0\n"
    "v_pk_max_i16 v194, v194, 0\n"
    "v_pk_max_i16 v195, v195, 0\n"
    "v_cvt_pk_bf16_f32 v200, v200, v201\n"
    "v_cvt_pk_bf16_f32 v201, v202, v203\n"
    "v_cvt_pk_bf16_f32 v202, v204, v205\n"
    "v_mfma_f32_32x32x16_bf16 v[112:127], a[124:127], a[172:175], v[112:127]\n"
    "ds_write_b128 v251, v[192:195] offset:23552\n"
    "v_cvt_pk_bf16_f32 v203, v206, v207\n"
    "v_pk_max_i16 v200, v200, 0\n"
    "v_mfma_f32_32x32x16_bf16 v[144:159], a[144:147], a[64:67], 0\n"
    "v_pk_max_i16 v201, v201, 0\n"
    "v_pk_max_i16 v202, v202, 0\n"
    "v_pk_max_i16 v203, v203, 0\n"
    "v_exp_f32_e32 v104, v104\n"
    "v_exp_f32_e32 v105, v105\n"
    "v_mfma_f32_32x32x16_bf16 v[144:159], a[148:151], a[68:71], v[144:159]\n"
    "ds_write_b128 v251, v[200:203] offset:24576\n"
    "v_exp_f32_e64 v106, v106\n"
    "v_mfma_f32_32x32x16_bf16 v[144:159], a[152:155], a[72:75], v[144:159]\n"
    "ds_read_b128 a[224:227], v254 offset:23552\n"
    "v_exp_f32_e32 v107, v107\n"
    "v_exp_f32_e32 v108, v108\n"
    "v_exp_f32_e64 v109, v109\n"
    "v_mfma_f32_32x32x16_bf16 v[144:159], a[156:159], a[76:79], v[144:159]\n"
    "v_exp_f32_e32 v110, v110\n"
    "v_exp_f32_e32 v111, v111\n"
    "v_cvt_pk_bf16_f32 v100, v104, v105\n"
    "v_cvt_pk_bf16_f32 v101, v106, v107\n"
    "v_mfma_f32_32x32x16_bf16 v[176:191], v[40:43], v[208:211], 0\n"
    "v_cvt_pk_bf16_f32 v102, v108, v109\n"
    "v_cvt_pk_bf16_f32 v103, v110, v111\n"
    "v_exp_f32_e32 v160, v160\n"
    "v_exp_f32_e32 v161, v161\n"
    "v_mfma_f32_32x32x16_bf16 v[128:143], v[76:79], v[100:103], v[128:143]\n"
    "v_cvt_pk_bf16_f32 v56, v144, v145\n"
    "v_cvt_pk_bf16_f32 v57, v146, v147\n"
    "v_cvt_pk_bf16_f32 v58, v148, v149\n"
    "v_cvt_pk_bf16_f32 v59, v150, v151\n"
    "v_cvt_pk_bf16_f32 v60, v152, v153\n"
    "v_cvt_pk_bf16_f32 v61, v154, v155\n"
    "v_smfmac_f32_16x16x64_bf16 v[220:223], v[238:241], v[96:103], v235\n"
    "buffer_load_dwordx4 v253, s[92:95], s96 offen offset:3072 nt lds\n"
    "v_cvt_pk_bf16_f32 v62, v156, v157\n"
    "v_mfma_f32_32x32x16_bf16 v[176:191], v[44:47], v[212:215], v[176:191]\n"
    "s_waitcnt lgkmcnt(0)\n"
    "v_cvt_pk_bf16_f32 v63, v158, v159\n"
    "v_exp_f32_e32 v162, v162\n"
    "v_exp_f32_e32 v163, v163\n"
    "v_exp_f32_e32 v164, v164\n"
    "v_mfma_f32_32x32x16_bf16 v[192:207], a[0:3], a[128:131], 0\n"
    "ds_read_b128 a[228:231], v254 offset:23680\n"
    "v_exp_f32_e32 v165, v165\n"
    "v_exp_f32_e32 v166, v166\n"
    "v_exp_f32_e64 v167, v167\n"
    "v_mfma_f32_32x32x16_bf16 v[192:207], a[4:7], a[132:135], v[192:207]\n"
    "v_cvt_pk_bf16_f32 v160, v160, v161\n"
    "v_cvt_pk_bf16_f32 v161, v162, v163\n"
    "v_cvt_pk_bf16_f32 v162, v164, v165\n"
    "v_cvt_pk_bf16_f32 v163, v166, v167\n"
    "v_exp_f32_e64 v168, v168\n"
    "v_mfma_f32_32x32x16_bf16 v[192:207], a[8:11], a[136:139], v[192:207]\n"
    "v_exp_f32_e32 v169, v169\n"
    "v_exp_f32_e32 v170, v170\n"
    "v_exp_f32_e64 v171, v171\n"
    "v_mfma_f32_32x32x16_bf16 v[128:143], v[80:83], v[160:163], v[128:143]\n"
    "v_exp_f32_e32 v172, v172\n"
    "v_exp_f32_e32 v173, v173\n"
    "v_exp_f32_e64 v174, v174\n"
    "v_mfma_f32_32x32x16_bf16 v[192:207], a[12:15], a[140:143], v[192:207]\n"
    "buffer_store_dwordx4 a[208:211], v242, s[44:47], 0 offen offset:0 nt\n"
    "v_exp_f32_e64 v175, v175\n"
    "v_cvt_pk_bf16_f32 v164, v168, v169\n"
    "v_cvt_pk_bf16_f32 v165, v170, v171\n"
    "v_cvt_pk_bf16_f32 v166, v172, v173\n"
    "v_cvt_pk_bf16_f32 v167, v174, v175\n"
    "v_exp_f32_e32 v176, v176\n"
    "v_exp_f32_e32 v177, v177\n"
    "v_mfma_f32_32x32x16_bf16 v[128:143], v[84:87], v[164:167], v[128:143]\n"
    "s_waitcnt lgkmcnt(0)\n"
    "v_exp_f32_e32 v178, v178\n"
    "v_exp_f32_e32 v179, v179\n"
    "v_exp_f32_e32 v180, v180\n"
    "v_smfmac_f32_16x16x64_bf16 v[220:223], v[238:241], v[160:167], v235\n"
    "v_exp_f32_e64 v181, v181\n"
    "v_mfma_f32_32x32x16_bf16 v[160:175], a[48:51], a[128:131], 0\n"
    "v_exp_f32_e32 v182, v182\n"
    "v_exp_f32_e32 v183, v183\n"
    "v_cvt_pk_bf16_f32 v176, v176, v177\n"
    "v_cvt_pk_bf16_f32 v177, v178, v179\n"
    "v_mfma_f32_32x32x16_bf16 v[160:175], a[52:55], a[132:135], v[160:175]\n"
    "ds_read_b128 a[232:235], v254 offset:23808\n"
    "v_cvt_pk_bf16_f32 v178, v180, v181\n"
    "v_cvt_pk_bf16_f32 v179, v182, v183\n"
    "v_cvt_pk_bf16_f32 v208, v192, v193\n"
    "v_cvt_pk_bf16_f32 v209, v194, v195\n"
    "v_cvt_pk_bf16_f32 v210, v196, v197\n"
    "v_mfma_f32_32x32x16_bf16 v[128:143], v[88:91], v[176:179], v[128:143]\n"
    "v_cvt_pk_bf16_f32 v211, v198, v199\n"
    "v_cvt_pk_bf16_f32 v212, v200, v201\n"
    "v_cvt_pk_bf16_f32 v213, v202, v203\n"
    "v_cvt_pk_bf16_f32 v214, v204, v205\n"
    "v_cvt_pk_bf16_f32 v215, v206, v207\n"
    "v_exp_f32_e64 v184, v184\n"
    "v_mfma_f32_32x32x16_bf16 v[96:111], v[0:3], v[208:211], 0\n"
    "v_exp_f32_e32 v185, v185\n"
    "v_exp_f32_e32 v186, v186\n"
    "v_exp_f32_e64 v187, v187\n"
    "v_mfma_f32_32x32x16_bf16 v[96:111], v[4:7], v[212:215], v[96:111]\n"
    "v_exp_f32_e32 v188, v188\n"
    "v_exp_f32_e32 v189, v189\n"
    "v_exp_f32_e64 v190, v190\n"
    "v_mfma_f32_16x16x32_bf16 v[216:219], a[176:179], v[208:211], 0\n"
    "v_exp_f32_e64 v191, v191\n"
    "v_mfma_f32_16x16x32_bf16 v[216:219], a[180:183], v[212:215], v[216:219]\n"
    "s_waitcnt lgkmcnt(0)\n"
    "v_cvt_pk_bf16_f32 v180, v184, v185\n"
    "v_cvt_pk_bf16_f32 v181, v186, v187\n"
    "v_mfma_f32_32x32x16_bf16 v[144:159], v[8:11], v[208:211], 0\n"
    "v_cvt_pk_bf16_f32 v182, v188, v189\n"
    "v_cvt_pk_bf16_f32 v183, v190, v191\n"
    "v_subrev_u32_e32 v243, s87, v243\n"
    "v_subrev_u32_e32 v244, s87, v244\n"
    "v_subrev_u32_e32 v245, s87, v245\n"
    "v_subrev_u32_e64 v246, s87, v246\n"
    "v_mfma_f32_32x32x16_bf16 v[128:143], v[92:95], v[180:183], v[128:143]\n"
    "ds_read_b128 a[236:239], v254 offset:23936\n"
    "v_exp_f32_e32 v216, v216\n"
    "v_exp_f32_e32 v217, v217\n"
    "v_exp_f32_e64 v96, v96\n"
    "v_smfmac_f32_16x16x64_bf16 v[220:223], v[238:241], v[176:183], v235\n"
    "v_exp_f32_e64 v97, v97\n"
    "v_mfma_f32_32x32x16_bf16 v[144:159], v[12:15], v[212:215], v[144:159]\n"
    "s_waitcnt vmcnt(10)\n"
    "ds_read_b128 a[160:163], v243 offset:8192\n"
    "v_add_u32_e32 v243, s86, v243\n"
    "ds_read_b128 a[164:167], v244 offset:8192\n"
    "v_add_u32_e64 v244, s86, v244\n"
    "v_mfma_f32_32x32x16_bf16 v[160:175], a[56:59], a[136:139], v[160:175]\n"
    "ds_read_b128 a[168:171], v245 offset:8192\n"
    "v_add_u32_e64 v245, s86, v245\n"
    "ds_read_b128 a[172:175], v246 offset:8192\n"
    "v_add_u32_e32 v246, s86, v246\n"
    "v_exp_f32_e32 v98, v98\n"
    "v_mfma_f32_32x32x16_bf16 v[160:175], a[60:63], a[140:143], v[160:175]\n"
    "buffer_store_dwordx4 a[212:215], v242, s[44:47], 0 offen offset:1024 nt\n"
    "v_exp_f32_e32 v99, v99\n"
    "v_exp_f32_e32 v100, v100\n"
    "v_exp_f32_e64 v101, v101\n"
    "v_mfma_f32_32x32x16_bf16 v[176:191], a[96:99], a[128:131], 0\n"
    "s_waitcnt lgkmcnt(4)\n"
    "v_exp_f32_e32 v102, v102\n"
    "v_exp_f32_e64 v103, v103\n"
    "v_cvt_pk_bf16_f32 v96, v96, v97\n"
    "v_cvt_pk_bf16_f32 v24, v160, v161\n"
    "v_mfma_f32_32x32x16_bf16 v[176:191], a[100:103], a[132:135], v[176:191]\n"
    "v_cvt_pk_bf16_f32 v25, v162, v163\n"
    "v_cvt_pk_bf16_f32 v26, v164, v165\n"
    "v_cvt_pk_bf16_f32 v27, v166, v167\n"
    "v_cvt_pk_bf16_f32 v28, v168, v169\n"
    "v_cvt_pk_bf16_f32 v29, v170, v171\n"
    "v_cvt_pk_bf16_f32 v30, v172, v173\n"
    "v_mfma_f32_32x32x16_bf16 v[176:191], a[104:107], a[136:139], v[176:191]\n"
    "s_waitcnt lgkmcnt(2)\n"
    "v_cvt_pk_bf16_f32 v31, v174, v175\n"
    "v_cvt_pk_bf16_f32 v97, v98, v99\n"
    "v_cvt_pk_bf16_f32 v98, v100, v101\n"
    "v_cvt_pk_bf16_f32 v99, v102, v103\n"
    "v_add_f32_e32 v236, v220, v221\n"
    "v_rcp_f32_e64 v232, v236\n"
    "v_mfma_f32_32x32x16_bf16 v[176:191], a[108:111], a[140:143], v[176:191]\n"
    "s_waitcnt lgkmcnt(0)\n"
    "v_fmac_f32_e32 v112, v128, v232\n"
    "v_fmac_f32_e32 v113, v129, v232\n"
    "v_fmac_f32_e32 v114, v130, v232\n"
    "v_fmac_f32_e32 v115, v131, v232\n"
    "v_fmac_f32_e32 v116, v132, v232\n"
    "v_fmac_f32_e64 v117, v133, v232\n"
    "v_mfma_f32_32x32x16_bf16 v[160:175], a[32:35], a[160:163], 0\n"
    "v_fmac_f32_e32 v118, v134, v232\n"
    "v_fmac_f32_e32 v119, v135, v232\n"
    "v_fmac_f32_e32 v120, v136, v232\n"
    "v_fmac_f32_e32 v121, v137, v232\n"
    "v_fmac_f32_e32 v122, v138, v232\n"
    "v_fmac_f32_e32 v123, v139, v232\n"
    "v_mfma_f32_32x32x16_bf16 v[160:175], a[36:39], a[164:167], v[160:175]\n"
    "v_fmac_f32_e32 v124, v140, v232\n"
    "v_fmac_f32_e32 v125, v141, v232\n"
    "v_fmac_f32_e32 v126, v142, v232\n"
    "v_fmac_f32_e32 v127, v143, v232\n"
    "v_maximum3_f32 v233, v233, v236, v232\n"
    "v_cvt_pk_bf16_f32 v230, v216, v217\n"
    "v_mfma_f32_32x32x16_bf16 v[160:175], a[40:43], a[168:171], v[160:175]\n"
    "v_mov_b32_e64 v226, v225\n"
    "v_cvt_pk_bf16_f32 v112, v112, v113\n"
    "v_cvt_pk_bf16_f32 v113, v114, v115\n"
    "v_cvt_pk_bf16_f32 v114, v116, v117\n"
    "v_cvt_pk_bf16_f32 v115, v118, v119\n"
    "v_pk_max_i16 v112, v112, 0\n"
    "v_mfma_f32_16x16x16_bf16 v[220:223], v[234:235], v[230:231], 0\n"
    "v_pk_max_i16 v113, v113, 0\n"
    "v_pk_max_i16 v114, v114, 0\n"
    "v_pk_max_i16 v115, v115, 0\n"
    "v_mfma_f32_32x32x8_bf16 v[192:207], v[224:225], v[230:231], 0\n"
    "v_cvt_pk_bf16_f32 v120, v120, v121\n"
    "v_cvt_pk_bf16_f32 v121, v122, v123\n"
    "v_cvt_pk_bf16_f32 v122, v124, v125\n"
    "v_cvt_pk_bf16_f32 v123, v126, v127\n"
    "v_pk_max_i16 v120, v120, 0\n"
    "v_pk_max_i16 v121, v121, 0\n"
    "v_pk_max_i16 v122, v122, 0\n"
    "v_mfma_f32_32x32x16_bf16 v[160:175], a[44:47], a[172:175], v[160:175]\n"
    "ds_write_b128 v251, v[112:115] offset:25600\n"
    "v_pk_max_i16 v123, v123, 0\n"
    "v_exp_f32_e64 v104, v104\n"
    "v_mfma_f32_32x32x16_bf16 v[192:207], v[48:51], v[96:99], v[192:207]\n"
    "ds_write_b128 v251, v[120:123] offset:26624\n"
    "v_exp_f32_e64 v105, v105\n"
    "v_mfma_f32_32x32x16_bf16 v[128:143], a[160:163], a[64:67], 0\n"
    "ds_read_b128 a[240:243], v254 offset:25600\n"
    "v_exp_f32_e32 v106, v106\n"
    "v_exp_f32_e32 v107, v107\n"
    "v_exp_f32_e64 v108, v108\n"
    "v_mfma_f32_32x32x16_bf16 v[128:143], a[164:167], a[68:71], v[128:143]\n"
    "buffer_store_dwordx4 a[216:219], v242, s[44:47], 0 offen offset:2048 nt\n"
    "v_exp_f32_e32 v109, v109\n"
    "v_exp_f32_e32 v110, v110\n"
    "v_exp_f32_e64 v111, v111\n"
    "v_mfma_f32_32x32x16_bf16 v[128:143], a[168:171], a[72:75], v[128:143]\n"
    "v_cvt_pk_bf16_f32 v100, v104, v105\n"
    "v_cvt_pk_bf16_f32 v101, v106, v107\n"
    "v_cvt_pk_bf16_f32 v102, v108, v109\n"
    "v_cvt_pk_bf16_f32 v103, v110, v111\n"
    "v_cvt_pk_bf16_f32 v16, v160, v161\n"
    "v_cvt_pk_bf16_f32 v17, v162, v163\n"
    "v_mfma_f32_32x32x16_bf16 v[192:207], v[52:55], v[100:103], v[192:207]\n"
    "v_cvt_pk_bf16_f32 v18, v164, v165\n"
    "v_cvt_pk_bf16_f32 v19, v166, v167\n"
    "v_cvt_pk_bf16_f32 v20, v168, v169\n"
    "v_cvt_pk_bf16_f32 v21, v170, v171\n"
    "v_cvt_pk_bf16_f32 v22, v172, v173\n"
    "v_cvt_pk_bf16_f32 v23, v174, v175\n"
    "v_smfmac_f32_16x16x64_bf16 v[220:223], v[238:241], v[96:103], v235\n"
    "ds_read_b128 a[244:247], v254 offset:25728\n"
    "v_exp_f32_e64 v144, v144\n"
    "v_mfma_f32_32x32x16_bf16 v[128:143], a[172:175], a[76:79], v[128:143]\n"
    "s_waitcnt lgkmcnt(1)\n"
    "v_exp_f32_e32 v145, v145\n"
    "v_exp_f32_e32 v146, v146\n"
    "v_exp_f32_e32 v147, v147\n"
    "v_mfma_f32_32x32x16_bf16 v[112:127], v[16:19], v[208:211], 0\n"
    "v_exp_f32_e32 v148, v148\n"
    "v_exp_f32_e32 v149, v149\n"
    "v_exp_f32_e64 v150, v150\n"
    "v_mfma_f32_32x32x16_bf16 v[112:127], v[20:23], v[212:215], v[112:127]\n"
    "v_cvt_pk_bf16_f32 v64, v128, v129\n"
    "v_cvt_pk_bf16_f32 v65, v130, v131\n"
    "v_cvt_pk_bf16_f32 v66, v132, v133\n"
    "v_cvt_pk_bf16_f32 v67, v134, v135\n"
    "v_cvt_pk_bf16_f32 v68, v136, v137\n"
    "v_cvt_pk_bf16_f32 v69, v138, v139\n"
    "v_mfma_f32_32x32x16_bf16 v[96:111], a[0:3], a[144:147], 0\n"
    "v_cvt_pk_bf16_f32 v70, v140, v141\n"
    "v_cvt_pk_bf16_f32 v71, v142, v143\n"
    "v_exp_f32_e64 v151, v151\n"
    "v_cvt_pk_bf16_f32 v144, v144, v145\n"
    "v_cvt_pk_bf16_f32 v145, v146, v147\n"
    "v_mfma_f32_32x32x16_bf16 v[96:111], a[4:7], a[148:151], v[96:111]\n"
    "s_waitcnt lgkmcnt(0)\n"
    "v_cvt_pk_bf16_f32 v146, v148, v149\n"
    "v_cvt_pk_bf16_f32 v147, v150, v151\n"
    "v_exp_f32_e32 v152, v152\n"
    "v_exp_f32_e64 v153, v153\n"
    "v_mfma_f32_32x32x16_bf16 v[192:207], v[56:59], v[144:147], v[192:207]\n"
    "v_exp_f32_e32 v154, v154\n"
    "v_exp_f32_e32 v155, v155\n"
    "v_exp_f32_e64 v156, v156\n"
    "v_mfma_f32_32x32x16_bf16 v[96:111], a[8:11], a[152:155], v[96:111]\n"
    "v_exp_f32_e32 v157, v157\n"
    "v_exp_f32_e32 v158, v158\n"
    "v_exp_f32_e64 v159, v159\n"
    "v_mfma_f32_32x32x16_bf16 v[96:111], a[12:15], a[156:159], v[96:111]\n"
    "v_cvt_pk_bf16_f32 v148, v152, v153\n"
    "v_cvt_pk_bf16_f32 v149, v154, v155\n"
    "v_cvt_pk_bf16_f32 v150, v156, v157\n"
    "v_cvt_pk_bf16_f32 v151, v158, v159\n"
    "v_exp_f32_e32 v112, v112\n"
    "v_exp_f32_e32 v113, v113\n"
    "v_mfma_f32_32x32x16_bf16 v[192:207], v[60:63], v[148:151], v[192:207]\n"
    "ds_read_b128 a[248:251], v254 offset:25856\n"
    "v_exp_f32_e32 v114, v114\n"
    "v_exp_f32_e32 v115, v115\n"
    "v_exp_f32_e64 v116, v116\n"
    "v_smfmac_f32_16x16x64_bf16 v[220:223], v[238:241], v[144:151], v235\n"
    "v_exp_f32_e64 v117, v117\n"
    "v_mfma_f32_32x32x16_bf16 v[144:159], a[48:51], a[144:147], 0\n"
    "buffer_store_dwordx4 a[220:223], v242, s[44:47], 0 offen offset:3072 nt\n"
    "v_exp_f32_e32 v118, v118\n"
    "v_exp_f32_e32 v119, v119\n"
    "v_cvt_pk_bf16_f32 v112, v112, v113\n"
    "v_mfma_f32_32x32x16_bf16 v[144:159], a[52:55], a[148:151], v[144:159]\n"
    "v_cvt_pk_bf16_f32 v113, v114, v115\n"
    "v_cvt_pk_bf16_f32 v114, v116, v117\n"
    "v_cvt_pk_bf16_f32 v115, v118, v119\n"
    "v_cvt_pk_bf16_f32 v208, v96, v97\n"
    "v_cvt_pk_bf16_f32 v209, v98, v99\n"
    "v_cvt_pk_bf16_f32 v210, v100, v101\n"
    "v_mfma_f32_32x32x16_bf16 v[192:207], v[64:67], v[112:115], v[192:207]\n"
    "v_cvt_pk_bf16_f32 v211, v102, v103\n"
    "v_cvt_pk_bf16_f32 v212, v104, v105\n"
    "v_cvt_pk_bf16_f32 v213, v106, v107\n"
    "v_cvt_pk_bf16_f32 v214, v108, v109\n"
    "v_cvt_pk_bf16_f32 v215, v110, v111\n"
    "v_exp_f32_e64 v120, v120\n"
    "v_mfma_f32_32x32x16_bf16 v[160:175], v[0:3], v[208:211], 0\n"
    "s_waitcnt lgkmcnt(0)\n"
    "v_exp_f32_e32 v121, v121\n"
    "v_exp_f32_e32 v122, v122\n"
    "v_exp_f32_e32 v123, v123\n"
    "v_mfma_f32_32x32x16_bf16 v[160:175], v[4:7], v[212:215], v[160:175]\n"
    "v_exp_f32_e32 v124, v124\n"
    "v_exp_f32_e32 v125, v125\n"
    "v_exp_f32_e64 v126, v126\n"
    "v_mfma_f32_16x16x32_bf16 v[216:219], a[176:179], v[208:211], 0\n"
    "v_exp_f32_e64 v127, v127\n"
    "v_mfma_f32_16x16x32_bf16 v[216:219], a[180:183], v[212:215], v[216:219]\n"
    "v_cvt_pk_bf16_f32 v116, v120, v121\n"
    "v_cvt_pk_bf16_f32 v117, v122, v123\n"
    "v_mfma_f32_32x32x16_bf16 v[128:143], v[8:11], v[208:211], 0\n"
    "v_cvt_pk_bf16_f32 v118, v124, v125\n"
    "v_cvt_pk_bf16_f32 v119, v126, v127\n"
    "v_exp_f32_e32 v160, v160\n"
    "v_exp_f32_e32 v161, v161\n"
    "v_mfma_f32_32x32x16_bf16 v[192:207], v[68:71], v[116:119], v[192:207]\n"
    "ds_read_b128 a[252:255], v254 offset:25984\n"
    "v_exp_f32_e32 v216, v216\n"
    "v_exp_f32_e32 v217, v217\n"
    "v_exp_f32_e64 v162, v162\n"
    "v_smfmac_f32_16x16x64_bf16 v[220:223], v[238:241], v[112:119], v235\n"
    "v_exp_f32_e64 v163, v163\n"
    "v_mfma_f32_32x32x16_bf16 v[128:143], v[12:15], v[212:215], v[128:143]\n"
    "v_exp_f32_e32 v164, v164\n"
    "v_exp_f32_e32 v165, v165\n"
    "v_exp_f32_e64 v166, v166\n"
    "v_mfma_f32_32x32x16_bf16 v[144:159], a[56:59], a[152:155], v[144:159]\n"
    "v_exp_f32_e64 v167, v167\n"
    "v_cvt_pk_bf16_f32 v160, v160, v161\n"
    "v_cvt_pk_bf16_f32 v161, v162, v163\n"
    "v_cvt_pk_bf16_f32 v162, v164, v165\n"
    "v_cvt_pk_bf16_f32 v163, v166, v167\n"
    "v_mfma_f32_32x32x16_bf16 v[144:159], a[60:63], a[156:159], v[144:159]\n"
    "v_add_f32_e32 v236, v220, v221\n"
    "v_rcp_f32_e32 v232, v236\n"
    "v_cvt_pk_bf16_f32 v230, v216, v217\n"
    "v_fmac_f32_e32 v176, v192, v232\n"
    "v_fmac_f32_e32 v177, v193, v232\n"
    "v_mfma_f32_16x16x16_bf16 v[220:223], v[234:235], v[230:231], 0\n"
    "v_fmac_f32_e32 v178, v194, v232\n"
    "v_fmac_f32_e32 v179, v195, v232\n"
    "v_fmac_f32_e64 v180, v196, v232\n"
    "v_mfma_f32_32x32x8_bf16 v[96:111], v[224:225], v[230:231], 0\n"
    "v_cvt_pk_bf16_f32 v32, v144, v145\n"
    "v_cvt_pk_bf16_f32 v33, v146, v147\n"
    "v_cvt_pk_bf16_f32 v34, v148, v149\n"
    "v_cvt_pk_bf16_f32 v35, v150, v151\n"
    "v_cvt_pk_bf16_f32 v36, v152, v153\n"
    "v_cvt_pk_bf16_f32 v37, v154, v155\n"
    "v_cvt_pk_bf16_f32 v38, v156, v157\n"
    "v_mfma_f32_32x32x16_bf16 v[112:127], a[96:99], a[144:147], 0\n"
    "s_waitcnt lgkmcnt(0)\n"
    "v_cvt_pk_bf16_f32 v39, v158, v159\n"
    "v_fmac_f32_e32 v181, v197, v232\n"
    "v_fmac_f32_e32 v182, v198, v232\n"
    "v_fmac_f32_e32 v183, v199, v232\n"
    "v_fmac_f32_e32 v184, v200, v232\n"
    "v_fmac_f32_e32 v185, v201, v232\n"
    "v_mfma_f32_32x32x16_bf16 v[96:111], v[48:51], v[160:163], v[96:111]\n"
    "buffer_store_dwordx4 a[224:227], v242, s[44:47], s96 offen offset:0 nt\n"
    "v_fmac_f32_e32 v186, v202, v232\n"
    "v_fmac_f32_e32 v187, v203, v232\n"
    "v_fmac_f32_e32 v188, v204, v232\n"
    "v_fmac_f32_e32 v189, v205, v232\n"
    "v_fmac_f32_e64 v190, v206, v232\n"
    "v_mfma_f32_32x32x16_bf16 v[112:127], a[100:103], a[148:151], v[112:127]\n"
    "v_fmac_f32_e64 v191, v207, v232\n"
    "v_maximum3_f32 v233, v233, v236, v232\n"
    "v_cvt_pk_bf16_f32 v176, v176, v177\n"
    "v_cvt_pk_bf16_f32 v177, v178, v179\n"
    "v_cvt_pk_bf16_f32 v178, v180, v181\n"
    "v_cvt_pk_bf16_f32 v179, v182, v183\n"
    "v_mfma_f32_32x32x16_bf16 v[112:127], a[104:107], a[152:155], v[112:127]\n"
    "v_pk_max_i16 v176, v176, 0\n"
    "v_pk_max_i16 v177, v177, 0\n"
    "v_pk_max_i16 v178, v178, 0\n"
    "v_pk_max_i16 v179, v179, 0\n"
    "v_cvt_pk_bf16_f32 v184, v184, v185\n"
    "v_cvt_pk_bf16_f32 v185, v186, v187\n"
    "v_mfma_f32_32x32x16_bf16 v[112:127], a[108:111], a[156:159], v[112:127]\n"
    "ds_write_b128 v251, v[176:179] offset:23552\n"
    "v_cvt_pk_bf16_f32 v186, v188, v189\n"
    "v_cvt_pk_bf16_f32 v187, v190, v191\n"
    "v_mfma_f32_32x32x16_bf16 v[144:159], a[48:51], a[160:163], 0\n"
    "v_pk_max_i16 v184, v184, 0\n"
    "v_pk_max_i16 v185, v185, 0\n"
    "v_pk_max_i16 v186, v186, 0\n"
    "v_pk_max_i16 v187, v187, 0\n"
    "v_exp_f32_e64 v168, v168\n"
    "v_mfma_f32_32x32x16_bf16 v[144:159], a[52:55], a[164:167], v[144:159]\n"
    "ds_write_b128 v251, v[184:187] offset:24576\n"
    "v_exp_f32_e64 v169, v169\n"
    "v_mfma_f32_32x32x16_bf16 v[144:159], a[56:59], a[168:171], v[144:159]\n"
    "v_exp_f32_e32 v170, v170\n"
    "v_exp_f32_e32 v171, v171\n"
    "v_exp_f32_e64 v172, v172\n"
    "v_mfma_f32_32x32x16_bf16 v[144:159], a[60:63], a[172:175], v[144:159]\n"
    "v_exp_f32_e32 v173, v173\n"
    "v_exp_f32_e32 v174, v174\n"
    "v_exp_f32_e64 v175, v175\n"
    "v_mfma_f32_32x32x16_bf16 v[192:207], v[16:19], v[208:211], 0\n"
    "v_cvt_pk_bf16_f32 v164, v168, v169\n"
    "v_cvt_pk_bf16_f32 v165, v170, v171\n"
    "v_cvt_pk_bf16_f32 v166, v172, v173\n"
    "v_cvt_pk_bf16_f32 v167, v174, v175\n"
    "v_cvt_pk_bf16_f32 v40, v144, v145\n"
    "v_cvt_pk_bf16_f32 v41, v146, v147\n"
    "v_mfma_f32_32x32x16_bf16 v[96:111], v[52:55], v[164:167], v[96:111]\n"
    "v_cvt_pk_bf16_f32 v42, v148, v149\n"
    "v_cvt_pk_bf16_f32 v43, v150, v151\n"
    "v_cvt_pk_bf16_f32 v44, v152, v153\n"
    "v_cvt_pk_bf16_f32 v45, v154, v155\n"
    "v_cvt_pk_bf16_f32 v46, v156, v157\n"
    "v_cvt_pk_bf16_f32 v47, v158, v159\n"
    "v_smfmac_f32_16x16x64_bf16 v[220:223], v[238:241], v[160:167], v235\n"
    "v_exp_f32_e64 v128, v128\n"
    "v_mfma_f32_32x32x16_bf16 v[192:207], v[20:23], v[212:215], v[192:207]\n"
    "v_exp_f32_e32 v129, v129\n"
    "v_exp_f32_e32 v130, v130\n"
    "v_exp_f32_e64 v131, v131\n"
    "v_mfma_f32_32x32x16_bf16 v[176:191], a[0:3], a[160:163], 0\n"
    "v_exp_f32_e32 v132, v132\n"
    "v_exp_f32_e32 v133, v133\n"
    "v_exp_f32_e64 v134, v134\n"
    "v_mfma_f32_32x32x16_bf16 v[176:191], a[4:7], a[164:167], v[176:191]\n"
    "v_exp_f32_e64 v135, v135\n"
    "v_cvt_pk_bf16_f32 v128, v128, v129\n"
    "v_cvt_pk_bf16_f32 v129, v130, v131\n"
    "v_cvt_pk_bf16_f32 v130, v132, v133\n"
    "v_cvt_pk_bf16_f32 v131, v134, v135\n"
    "v_mfma_f32_32x32x16_bf16 v[176:191], a[8:11], a[168:171], v[176:191]\n"
    "buffer_store_dwordx4 a[228:231], v242, s[44:47], s96 offen offset:1024 nt\n"
    "v_exp_f32_e32 v136, v136\n"
    "v_exp_f32_e32 v137, v137\n"
    "v_exp_f32_e64 v138, v138\n"
    "v_mfma_f32_32x32x16_bf16 v[96:111], v[56:59], v[128:131], v[96:111]\n"
    "v_exp_f32_e32 v139, v139\n"
    "v_exp_f32_e32 v140, v140\n"
    "v_exp_f32_e64 v141, v141\n"
    "v_mfma_f32_32x32x16_bf16 v[176:191], a[12:15], a[172:175], v[176:191]\n"
    "v_exp_f32_e32 v142, v142\n"
    "v_exp_f32_e32 v143, v143\n"
    "v_cvt_pk_bf16_f32 v132, v136, v137\n"
    "v_cvt_pk_bf16_f32 v133, v138, v139\n"
    "v_mfma_f32_16x16x32_bf16 v[136:139], v[16:19], a[192:195], 0\n"
    "v_cvt_pk_bf16_f32 v134, v140, v141\n"
    "v_cvt_pk_bf16_f32 v135, v142, v143\n"
    "v_mfma_f32_16x16x32_bf16 v[140:143], a[176:179], a[192:195], 0\n"
    "v_exp_f32_e64 v192, v192\n"
    "v_mfma_f32_32x32x16_bf16 v[96:111], v[60:63], v[132:135], v[96:111]\n"
    "v_exp_f32_e32 v193, v193\n"
    "v_exp_f32_e32 v194, v194\n"
    "v_exp_f32_e64 v195, v195\n"
    "v_smfmac_f32_16x16x64_bf16 v[220:223], v[238:241], v[128:135], v235\n"
    "v_exp_f32_e64 v196, v196\n"
    "v_mfma_f32_16x16x32_bf16 v[128:131], v[0:3], a[192:195], 0\n"
    "v_exp_f32_e64 v197, v197\n"
    "v_mfma_f32_16x16x32_bf16 v[132:135], v[8:11], a[192:195], 0\n"
    "v_exp_f32_e64 v198, v198\n"
    "v_mfma_f32_16x16x32_bf16 v[128:131], v[4:7], a[196:199], v[128:131]\n"
    "v_exp_f32_e64 v199, v199\n"
    "v_mfma_f32_16x16x32_bf16 v[132:135], v[12:15], a[196:199], v[132:135]\n"
    "v_cvt_pk_bf16_f32 v192, v192, v193\n"
    "v_cvt_pk_bf16_f32 v193, v194, v195\n"
    "v_mfma_f32_16x16x32_bf16 v[136:139], v[20:23], a[196:199], v[136:139]\n"
    "v_cvt_pk_bf16_f32 v194, v196, v197\n"
    "v_cvt_pk_bf16_f32 v195, v198, v199\n"
    "v_mfma_f32_16x16x32_bf16 v[140:143], a[180:183], a[196:199], v[140:143]\n"
    "v_cvt_pk_bf16_f32 v208, v176, v177\n"
    "v_cvt_pk_bf16_f32 v209, v178, v179\n"
    "v_mfma_f32_32x32x16_bf16 v[96:111], v[64:67], v[192:195], v[96:111]\n"
    "v_cvt_pk_bf16_f32 v210, v180, v181\n"
    "v_cvt_pk_bf16_f32 v211, v182, v183\n"
    "v_cvt_pk_bf16_f32 v212, v184, v185\n"
    "v_cvt_pk_bf16_f32 v213, v186, v187\n"
    "v_cvt_pk_bf16_f32 v214, v188, v189\n"
    "v_cvt_pk_bf16_f32 v215, v190, v191\n"
    "v_mfma_f32_32x32x16_bf16 v[160:175], v[0:3], v[208:211], 0\n"
    "v_exp_f32_e32 v200, v200\n"
    "v_exp_f32_e32 v201, v201\n"
    "v_exp_f32_e64 v202, v202\n"
    "v_mfma_f32_32x32x16_bf16 v[160:175], v[4:7], v[212:215], v[160:175]\n"
    "v_exp_f32_e32 v203, v203\n"
    "v_exp_f32_e32 v204, v204\n"
    "v_exp_f32_e64 v205, v205\n"
    "v_mfma_f32_16x16x32_bf16 v[216:219], a[176:179], v[208:211], 0\n"
    "v_exp_f32_e64 v206, v206\n"
    "v_mfma_f32_16x16x32_bf16 v[216:219], a[180:183], v[212:215], v[216:219]\n"
    "v_exp_f32_e64 v207, v207\n"
    "v_mfma_f32_32x32x16_bf16 v[144:159], v[8:11], v[208:211], 0\n"
    "v_cvt_pk_bf16_f32 v196, v200, v201\n"
    "v_cvt_pk_bf16_f32 v197, v202, v203\n"
    "v_cvt_pk_bf16_f32 v198, v204, v205\n"
    "v_cvt_pk_bf16_f32 v199, v206, v207\n"
    "v_exp_f32_e64 v216, v216\n"
    "v_mfma_f32_32x32x16_bf16 v[144:159], v[12:15], v[212:215], v[144:159]\n"
    "s_add_u32 m0, s52, s97\n"
    "v_exp_f32_e32 v217, v217\n"
    "v_exp_f32_e32 v160, v160\n"
    "v_exp_f32_e32 v161, v161\n"
    "v_mfma_f32_32x32x16_bf16 v[96:111], v[68:71], v[196:199], v[96:111]\n"
    "buffer_load_dwordx4 v252, s[88:91], s97 offen offset:0 nt lds\n"
    "v_exp_f32_e32 v162, v162\n"
    "v_exp_f32_e32 v163, v163\n"
    "v_exp_f32_e64 v164, v164\n"
    "v_smfmac_f32_16x16x64_bf16 v[220:223], v[238:241], v[192:199], v235\n"
    "buffer_store_dwordx4 a[232:235], v242, s[44:47], s96 offen offset:2048 nt\n"
    "v_exp_f32_e64 v165, v165\n"
    "v_mfma_f32_16x16x32_bf16 v[128:131], v[24:27], a[200:203], v[128:131]\n"
    "v_exp_f32_e64 v166, v166\n"
    "v_mfma_f32_16x16x32_bf16 v[132:135], v[32:35], a[200:203], v[132:135]\n"
    "v_exp_f32_e64 v167, v167\n"
    "v_mfma_f32_16x16x32_bf16 v[136:139], v[40:43], a[200:203], v[136:139]\n"
    "v_cvt_pk_bf16_f32 v160, v160, v161\n"
    "v_cvt_pk_bf16_f32 v161, v162, v163\n"
    "v_mfma_f32_16x16x32_bf16 v[140:143], a[184:187], a[200:203], v[140:143]\n"
    "v_cvt_pk_bf16_f32 v162, v164, v165\n"
    "v_cvt_pk_bf16_f32 v163, v166, v167\n"
    "v_mfma_f32_16x16x32_bf16 v[128:131], v[28:31], a[204:207], v[128:131]\n"
    "v_add_f32_e32 v236, v220, v221\n"
    "v_rcp_f32_e32 v232, v236\n"
    "v_mfma_f32_16x16x32_bf16 v[132:135], v[36:39], a[204:207], v[132:135]\n"
    "buffer_load_dwordx4 v253, s[88:91], s97 offen offset:1024 nt lds\n"
    "v_fmac_f32_e32 v112, v96, v232\n"
    "v_fmac_f32_e32 v113, v97, v232\n"
    "v_mfma_f32_16x16x32_bf16 v[136:139], v[44:47], a[204:207], v[136:139]\n"
    "v_fmac_f32_e32 v114, v98, v232\n"
    "v_fmac_f32_e32 v115, v99, v232\n"
    "v_mfma_f32_16x16x32_bf16 v[140:143], a[188:191], a[204:207], v[140:143]\n"
    "v_fmac_f32_e32 v116, v100, v232\n"
    "v_fmac_f32_e32 v117, v101, v232\n"
    "v_mfma_f32_32x32x16_bf16 v[176:191], a[128:131], a[80:83], 0\n"
    "v_fmac_f32_e32 v118, v102, v232\n"
    "v_fmac_f32_e32 v119, v103, v232\n"
    "v_fmac_f32_e32 v120, v104, v232\n"
    "v_fmac_f32_e32 v121, v105, v232\n"
    "v_fmac_f32_e32 v122, v106, v232\n"
    "v_fmac_f32_e32 v123, v107, v232\n"
    "v_mfma_f32_32x32x16_bf16 v[176:191], a[132:135], a[84:87], v[176:191]\n"
    "v_fmac_f32_e32 v124, v108, v232\n"
    "v_fmac_f32_e32 v125, v109, v232\n"
    "v_fmac_f32_e32 v126, v110, v232\n"
    "v_fmac_f32_e32 v127, v111, v232\n"
    "v_maximum3_f32 v233, v233, v236, v232\n"
    "v_cvt_pk_bf16_f32 v230, v216, v217\n"
    "v_mfma_f32_32x32x16_bf16 v[176:191], a[136:139], a[88:91], v[176:191]\n"
    "v_cvt_pk_bf16_f32 v112, v112, v113\n"
    "v_cvt_pk_bf16_f32 v113, v114, v115\n"
    "v_cvt_pk_bf16_f32 v114, v116, v117\n"
    "v_cvt_pk_bf16_f32 v115, v118, v119\n"
    "v_pk_max_i16 v112, v112, 0\n"
    "v_pk_max_i16 v113, v113, 0\n"
    "v_mfma_f32_32x32x16_bf16 v[176:191], a[140:143], a[92:95], v[176:191]\n"
    "v_pk_max_i16 v114, v114, 0\n"
    "v_pk_max_i16 v115, v115, 0\n"
    "v_cvt_pk_bf16_f32 v120, v120, v121\n"
    "v_cvt_pk_bf16_f32 v121, v122, v123\n"
    "v_cvt_pk_bf16_f32 v122, v124, v125\n"
    "v_cvt_pk_bf16_f32 v123, v126, v127\n"
    "v_mfma_f32_16x16x16_bf16 v[220:223], v[234:235], v[230:231], 0\n"
    "v_pk_max_i16 v120, v120, 0\n"
    "v_pk_max_i16 v121, v121, 0\n"
    "v_pk_max_i16 v122, v122, 0\n"
    "v_mfma_f32_32x32x8_bf16 v[192:207], v[224:225], v[230:231], 0\n"
    "v_cvt_pk_bf16_f32 v72, v176, v177\n"
    "v_cvt_pk_bf16_f32 v73, v178, v179\n"
    "v_cvt_pk_bf16_f32 v74, v180, v181\n"
    "v_cvt_pk_bf16_f32 v75, v182, v183\n"
    "v_cvt_pk_bf16_f32 v76, v184, v185\n"
    "v_cvt_pk_bf16_f32 v77, v186, v187\n"
    "v_cvt_pk_bf16_f32 v78, v188, v189\n"
    "v_mfma_f32_32x32x16_bf16 v[96:111], a[144:147], a[80:83], 0\n"
    "ds_write_b128 v251, v[112:115] offset:25600\n"
    "v_cvt_pk_bf16_f32 v79, v190, v191\n"
    "v_pk_max_i16 v123, v123, 0\n"
    "v_mfma_f32_32x32x16_bf16 v[192:207], v[48:51], v[160:163], v[192:207]\n"
    "ds_write_b128 v251, v[120:123] offset:26624\n"
    "v_exp_f32_e64 v168, v168\n"
    "v_mfma_f32_32x32x16_bf16 v[176:191], a[96:99], a[160:163], 0\n"
    "buffer_store_dwordx4 a[236:239], v242, s[44:47], s96 offen offset:3072 nt\n"
    "v_exp_f32_e32 v169, v169\n"
    "v_exp_f32_e32 v170, v170\n"
    "v_exp_f32_e64 v171, v171\n"
    "v_mfma_f32_32x32x16_bf16 v[176:191], a[100:103], a[164:167], v[176:191]\n"
    "v_exp_f32_e32 v172, v172\n"
    "v_exp_f32_e32 v173, v173\n"
    "v_exp_f32_e64 v174, v174\n"
    "v_mfma_f32_32x32x16_bf16 v[176:191], a[104:107], a[168:171], v[176:191]\n"
    "v_exp_f32_e64 v175, v175\n"
    "v_cvt_pk_bf16_f32 v164, v168, v169\n"
    "v_cvt_pk_bf16_f32 v165, v170, v171\n"
    "v_cvt_pk_bf16_f32 v166, v172, v173\n"
    "v_cvt_pk_bf16_f32 v167, v174, v175\n"
    "v_mfma_f32_32x32x16_bf16 v[176:191], a[108:111], a[172:175], v[176:191]\n"
    "buffer_load_dwordx4 v252, s[88:91], s97 offen offset:2048 nt lds\n"
    "v_exp_f32_e32 v128, v128\n"
    "v_exp_f32_e32 v129, v129\n"
    "v_exp_f32_e64 v130, v130\n"
    "v_mfma_f32_32x32x16_bf16 v[192:207], v[52:55], v[164:167], v[192:207]\n"
    "v_exp_f32_e32 v131, v131\n"
    "v_exp_f32_e32 v132, v132\n"
    "v_exp_f32_e64 v133, v133\n"
    "v_smfmac_f32_16x16x64_bf16 v[220:223], v[238:241], v[160:167], v235\n"
    "v_exp_f32_e64 v134, v134\n"
    "v_mfma_f32_32x32x16_bf16 v[96:111], a[148:151], a[84:87], v[96:111]\n"
    "v_exp_f32_e32 v135, v135\n"
    "v_exp_f32_e32 v136, v136\n"
    "v_exp_f32_e64 v137, v137\n"
    "v_mfma_f32_32x32x16_bf16 v[96:111], a[152:155], a[88:91], v[96:111]\n"
    "v_exp_f32_e32 v138, v138\n"
    "v_exp_f32_e32 v139, v139\n"
    "v_exp_f32_e64 v140, v140\n"
    "v_mfma_f32_32x32x16_bf16 v[96:111], a[156:159], a[92:95], v[96:111]\n"
    "v_exp_f32_e64 v141, v141\n"
    "v_cndmask_b32_e64 v140, 0, v140, s[54:55]\n"
    "v_cndmask_b32_e64 v141, 0, v141, s[54:55]\n"
    "v_add_f32_e32 v142, v140, v141\n"
    "v_mul_f32_e32 v142, 0.5, v142\n"
    "v_mfma_f32_32x32x16_bf16 v[112:127], v[16:19], v[208:211], 0\n"
    "v_add_f32_e32 v142, v128, v142\n"
    "v_add_f32_e32 v142, v129, v142\n"
    "v_add_f32_e32 v142, v130, v142\n"
    "v_add_f32_e32 v142, v131, v142\n"
    "v_add_f32_e32 v142, v132, v142\n"
    "v_add_f32_e32 v142, v133, v142\n"
    "v_mfma_f32_32x32x16_bf16 v[112:127], v[20:23], v[212:215], v[112:127]\n"
    "v_add_f32_e32 v142, v134, v142\n"
    "v_add_f32_e32 v142, v135, v142\n"
    "v_add_f32_e32 v142, v136, v142\n"
    "v_add_f32_e32 v142, v137, v142\n"
    "v_add_f32_e32 v142, v138, v142\n"
    "v_add_f32_e32 v142, v139, v142\n"
    "v_mfma_f32_32x32x16_bf16 v[160:175], a[16:19], a[128:131], 0\n"
    "v_cvt_pk_bf16_f32 v128, v128, v129\n"
    "v_cvt_pk_bf16_f32 v129, v130, v131\n"
    "v_cvt_pk_bf16_f32 v132, v132, v133\n"
    "v_cvt_pk_bf16_f32 v133, v134, v135\n"
    "v_cvt_pk_bf16_f32 v136, v136, v137\n"
    "v_cvt_pk_bf16_f32 v137, v138, v139\n"
    "v_mfma_f32_32x32x16_bf16 v[160:175], a[20:23], a[132:135], v[160:175]\n"
    "ds_write_b64 v250, v[128:129] offset:0\n"
    "v_cvt_pk_bf16_f32 v140, v140, v141\n"
    "v_add_f32_dpp v142, v142, v142 quad_perm:[1,0,3,2] row_mask:0xf bank_mask:0xf\n"
    "v_mfma_f32_32x32x16_bf16 v[160:175], a[24:27], a[136:139], v[160:175]\n"
    "ds_write_b64 v250, v[132:133] offset:576\n"
    "v_mov_b32_e32 v143, v142\n"
    "v_mov_b32_e32 v139, 0\n"
    "v_mfma_f32_32x32x16_bf16 v[160:175], a[28:31], a[140:143], v[160:175]\n"
    "ds_write_b64 v250, v[136:137] offset:1152\n"
    "v_permlane16_swap_b32_e32 v142, v143\n"
    "v_add_f32_e32 v142, v142, v143\n"
    "v_mov_b32_e32 v143, v142\n"
    "v_mov_b32_e32 v137, 0\n"
    "v_cndmask_b32_e64 v136, 0, v140, s[56:57]\n"
    "v_permlane32_swap_b32_e32 v142, v143\n"
    "v_add_f32_e32 v143, v142, v143\n"
    "v_mfma_f32_16x16x16_bf16 v[128:131], v[224:225], v[136:137], 0\n"
    "v_rcp_f32_e64 v142, v143\n"
    "v_cndmask_b32_e64 v138, v140, 0, s[56:57]\n"
    "v_maximum3_f32 v233, v233, v143, v142\n"
    "v_cvt_pk_bf16_f32 v80, v96, v97\n"
    "v_mfma_f32_16x16x16_bf16 v[128:131], v[226:227], v[138:139], v[128:131]\n"
    "v_cvt_pk_bf16_f32 v81, v98, v99\n"
    "v_cvt_pk_bf16_f32 v82, v100, v101\n"
    "v_cvt_pk_bf16_f32 v83, v102, v103\n"
    "v_cvt_pk_bf16_f32 v84, v104, v105\n"
    "v_cvt_pk_bf16_f32 v85, v106, v107\n"
    "v_cvt_pk_bf16_f32 v86, v108, v109\n"
    "v_cvt_pk_bf16_f32 v87, v110, v111\n"
    "v_mfma_f32_32x32x16_bf16 v[96:111], a[160:163], a[80:83], 0\n"
    "buffer_load_dwordx4 v253, s[88:91], s97 offen offset:3072 nt lds\n"
    "v_exp_f32_e32 v144, v144\n"
    "v_exp_f32_e32 v145, v145\n"
    "v_exp_f32_e64 v146, v146\n"
    "v_mfma_f32_32x32x16_bf16 v[96:111], a[164:167], a[84:87], v[96:111]\n"
    "ds_read_b64 v[134:135], v250 offset:1728\n"
    "v_exp_f32_e32 v147, v147\n"
    "v_exp_f32_e32 v148, v148\n"
    "v_exp_f32_e64 v149, v149\n"
    "v_mfma_f32_32x32x16_bf16 v[96:111], a[168:171], a[88:91], v[96:111]\n"
    "ds_read_b128 a[192:195], v247 offset:0\n"
    "v_exp_f32_e32 v150, v150\n"
    "v_exp_f32_e32 v151, v151\n"
    "v_cvt_pk_bf16_f32 v144, v144, v145\n"
    "v_mfma_f32_32x32x16_bf16 v[96:111], a[172:175], a[92:95], v[96:111]\n"
    "ds_read_b128 a[196:199], v247 offset:32\n"
    "v_cvt_pk_bf16_f32 v145, v146, v147\n"
    "v_cvt_pk_bf16_f32 v146, v148, v149\n"
    "v_cvt_pk_bf16_f32 v147, v150, v151\n"
    "v_exp_f32_e32 v152, v152\n"
    "v_exp_f32_e32 v153, v153\n"
    "v_mfma_f32_32x32x16_bf16 v[192:207], v[56:59], v[144:147], v[192:207]\n"
    "ds_read_b128 a[200:203], v247 offset:576\n"
    "v_exp_f32_e32 v154, v154\n"
    "v_exp_f32_e32 v155, v155\n"
    "v_exp_f32_e32 v156, v156\n"
    "v_exp_f32_e32 v157, v157\n"
    "v_exp_f32_e32 v158, v158\n"
    "v_exp_f32_e32 v159, v159\n"
    "v_cvt_pk_bf16_f32 v148, v152, v153\n"
    "v_cvt_pk_bf16_f32 v149, v154, v155\n"
    "v_cvt_pk_bf16_f32 v150, v156, v157\n"
    "v_cvt_pk_bf16_f32 v151, v158, v159\n"
    "s_waitcnt lgkmcnt(2)\n"
    "v_mfma_f32_16x16x32_bf16 v[128:131], v[48:51], a[192:195], v[128:131]\n"
    "ds_read_b128 a[204:207], v247 offset:608\n"
    "v_cvt_pk_bf16_f32 v88, v96, v97\n"
    "v_mfma_f32_32x32x16_bf16 v[192:207], v[60:63], v[148:151], v[192:207]\n"
    "ds_read_b128 a[192:195], v247 offset:1152\n"
    "v_cvt_pk_bf16_f32 v89, v98, v99\n"
    "v_cvt_pk_bf16_f32 v90, v100, v101\n"
    "v_cvt_pk_bf16_f32 v91, v102, v103\n"
    "v_cvt_pk_bf16_f32 v92, v104, v105\n"
    "v_cvt_pk_bf16_f32 v93, v106, v107\n"
    "v_smfmac_f32_16x16x64_bf16 v[220:223], v[238:241], v[144:151], v235\n"
    "buffer_store_dwordx4 a[240:243], v242, s[44:47], s97 offen offset:0 nt\n"
    "v_cvt_pk_bf16_f32 v94, v108, v109\n"
    "s_waitcnt lgkmcnt(3)\n"
    "v_mfma_f32_16x16x32_bf16 v[128:131], v[52:55], a[196:199], v[128:131]\n"
    "ds_read_b128 a[196:199], v247 offset:1184\n"
    "v_cvt_pk_bf16_f32 v95, v110, v111\n"
    "s_waitcnt lgkmcnt(3)\n"
    "v_mfma_f32_16x16x32_bf16 v[128:131], v[56:59], a[200:203], v[128:131]\n"
    "ds_read_b128 a[200:203], v248 offset:64\n"
    "v_exp_f32_e32 v112, v112\n"
    "v_exp_f32_e32 v113, v113\n"
    "v_exp_f32_e32 v114, v114\n"
    "v_exp_f32_e32 v115, v115\n"
    "v_exp_f32_e32 v116, v116\n"
    "v_exp_f32_e32 v117, v117\n"
    "s_waitcnt lgkmcnt(3)\n"
    "v_mfma_f32_16x16x32_bf16 v[128:131], v[60:63], a[204:207], v[128:131]\n"
    "ds_read_b128 a[204:207], v248 offset:96\n"
    "v_exp_f32_e32 v118, v118\n"
    "s_waitcnt lgkmcnt(3)\n"
    "v_mfma_f32_16x16x32_bf16 v[128:131], v[64:67], a[192:195], v[128:131]\n"
    "ds_read_b128 a[192:195], v248 offset:640\n"
    "v_exp_f32_e64 v119, v119\n"
    "v_cvt_pk_bf16_f32 v112, v112, v113\n"
    "v_cvt_pk_bf16_f32 v113, v114, v115\n"
    "v_cvt_pk_bf16_f32 v114, v116, v117\n"
    "v_cvt_pk_bf16_f32 v115, v118, v119\n"
    "v_cvt_pk_bf16_f32 v208, v160, v161\n"
    "s_waitcnt lgkmcnt(3)\n"
    "v_mfma_f32_16x16x32_bf16 v[128:131], v[68:71], a[196:199], v[128:131]\n"
    "ds_read_b128 a[196:199], v248 offset:672\n"
    "v_cvt_pk_bf16_f32 v209, v162, v163\n"
    "v_mfma_f32_32x32x16_bf16 v[192:207], v[64:67], v[112:115], v[192:207]\n"
    "buffer_store_dwordx4 a[244:247], v242, s[44:47], s97 offen offset:1024 nt\n"
    "v_cvt_pk_bf16_f32 v210, v164, v165\n"
    "v_cvt_pk_bf16_f32 v211, v166, v167\n"
    "v_cvt_pk_bf16_f32 v212, v168, v169\n"
    "v_cvt_pk_bf16_f32 v213, v170, v171\n"
    "v_cvt_pk_bf16_f32 v214, v172, v173\n"
    "v_mfma_f32_32x32x16_bf16 v[144:159], v[24:27], v[208:211], 0\n"
    "s_waitcnt lgkmcnt(3)\n"
    "v_cvt_pk_bf16_f32 v215, v174, v175\n"
    "v_exp_f32_e32 v120, v120\n"
    "v_exp_f32_e32 v121, v121\n"
    "v_exp_f32_e64 v122, v122\n"
    "v_mfma_f32_32x32x16_bf16 v[144:159], v[28:31], v[212:215], v[144:159]\n"
    "s_waitcnt lgkmcnt(2)\n"
    "v_exp_f32_e32 v123, v123\n"
    "v_exp_f32_e32 v124, v124\n"
    "v_exp_f32_e32 v125, v125\n"
    "v_mfma_f32_16x16x32_bf16 v[216:219], a[184:187], v[208:211], 0\n"
    "s_waitcnt lgkmcnt(1)\n"
    "v_exp_f32_e32 v126, v126\n"
    "v_mfma_f32_16x16x32_bf16 v[216:219], a[188:191], v[212:215], v[216:219]\n"
    "v_exp_f32_e64 v127, v127\n"
    "v_mfma_f32_32x32x16_bf16 v[96:111], v[32:35], v[208:211], 0\n"
    "s_waitcnt lgkmcnt(0)\n"
    "v_cvt_pk_bf16_f32 v116, v120, v121\n"
    "v_cvt_pk_bf16_f32 v117, v122, v123\n"
    "v_cvt_pk_bf16_f32 v118, v124, v125\n"
    "v_cvt_pk_bf16_f32 v119, v126, v127\n"
    "v_lshlrev_b32_e32 v132, 16, v134\n"
    "v_and_b32_e64 v133, s61, v134\n"
    "v_mfma_f32_32x32x16_bf16 v[192:207], v[68:71], v[116:119], v[192:207]\n"
    "v_lshlrev_b32_e32 v134, 16, v135\n"
    "v_and_b32_e32 v135, s61, v135\n"
    "v_exp_f32_e32 v216, v216\n"
    "v_exp_f32_e32 v217, v217\n"
    "v_smfmac_f32_16x16x64_bf16 v[220:223], v[238:241], v[112:119], v235\n"
    "v_exp_f32_e64 v144, v144\n"
    "v_mfma_f32_32x32x16_bf16 v[96:111], v[36:39], v[212:215], v[96:111]\n"
    "v_exp_f32_e32 v145, v145\n"
    "v_exp_f32_e32 v146, v146\n"
    "v_exp_f32_e64 v147, v147\n"
    "v_mfma_f32_16x16x32_bf16 v[128:131], v[72:75], a[200:203], v[128:131]\n"
    "ds_read_b128 a[200:203], v248 offset:1216\n"
    "v_exp_f32_e64 v148, v148\n"
    "v_mfma_f32_16x16x32_bf16 v[128:131], v[76:79], a[204:207], v[128:131]\n"
    "ds_read_b128 a[204:207], v248 offset:1248\n"
    "v_exp_f32_e64 v149, v149\n"
    "v_mfma_f32_16x16x32_bf16 v[128:131], v[80:83], a[192:195], v[128:131]\n"
    "buffer_store_dwordx4 a[248:251], v242, s[44:47], s97 offen offset:2048 nt\n"
    "v_exp_f32_e64 v150, v150\n"
    "v_mfma_f32_16x16x32_bf16 v[128:131], v[84:87], a[196:199], v[128:131]\n"
    "v_exp_f32_e64 v151, v151\n"
    "v_mfma_f32_32x32x16_bf16 v[160:175], v[40:43], v[208:211], 0\n"
    "v_cvt_pk_bf16_f32 v144, v144, v145\n"
    "v_cvt_pk_bf16_f32 v145, v146, v147\n"
    "v_cvt_pk_bf16_f32 v146, v148, v149\n"
    "v_cvt_pk_bf16_f32 v147, v150, v151\n"
    "v_add_f32_e32 v236, v220, v221\n"
    "v_rcp_f32_e32 v232, v236\n"
    "v_mfma_f32_32x32x16_bf16 v[160:175], v[44:47], v[212:215], v[160:175]\n"
    "v_fmac_f32_e32 v176, v192, v232\n"
    "v_fmac_f32_e32 v177, v193, v232\n"
    "v_fmac_f32_e32 v178, v194, v232\n"
    "v_fmac_f32_e32 v179, v195, v232\n"
    "v_fmac_f32_e32 v180, v196, v232\n"
    "v_fmac_f32_e32 v181, v197, v232\n"
    "v_fmac_f32_e32 v182, v198, v232\n"
    "s_waitcnt lgkmcnt(1)\n"
    "v_mfma_f32_16x16x32_bf16 v[128:131], v[88:91], a[200:203], v[128:131]\n"
    "v_fmac_f32_e32 v183, v199, v232\n"
    "v_fmac_f32_e64 v184, v200, v232\n"
    "s_waitcnt lgkmcnt(0)\n"
    "v_mfma_f32_16x16x32_bf16 v[128:131], v[92:95], a[204:207], v[128:131]\n"
    "v_fmac_f32_e32 v185, v201, v232\n"
    "v_fmac_f32_e32 v186, v202, v232\n"
    "v_fmac_f32_e32 v187, v203, v232\n"
    "v_fmac_f32_e32 v188, v204, v232\n"
    "v_fmac_f32_e32 v189, v205, v232\n"
    "v_fmac_f32_e32 v190, v206, v232\n"
    "v_fmac_f32_e64 v191, v207, v232\n"
    "v_mfma_f32_32x32x16_bf16 v[192:207], a[16:19], a[144:147], 0\n"
    "v_fmac_f32_e32 v132, v128, v142\n"
    "v_fmac_f32_e32 v133, v129, v142\n"
    "v_fmac_f32_e32 v134, v130, v142\n"
    "v_fmac_f32_e32 v135, v131, v142\n"
    "v_cvt_pk_bf16_f32 v132, v132, v133\n"
    "v_cvt_pk_bf16_f32 v133, v134, v135\n"
    "v_mfma_f32_32x32x16_bf16 v[192:207], a[20:23], a[148:151], v[192:207]\n"
    "v_pk_max_i16 v132, v132, 0\n"
    "v_pk_max_i16 v133, v133, 0\n"
    "v_maximum3_f32 v233, v233, v236, v232\n"
    "v_cvt_pk_bf16_f32 v230, v216, v217\n"
    "v_cvt_pk_bf16_f32 v176, v176, v177\n"
    "v_cvt_pk_bf16_f32 v177, v178, v179\n"
    "v_mfma_f32_16x16x16_bf16 v[220:223], v[234:235], v[230:231], 0\n"
    "v_cvt_pk_bf16_f32 v178, v180, v181\n"
    "v_cvt_pk_bf16_f32 v179, v182, v183\n"
    "buffer_store_dwordx2 v[132:133], v237, s[44:47], s99 offen nt\n"
    "v_mfma_f32_32x32x8_bf16 v[112:127], v[226:227], v[230:231], 0\n"
    "v_pk_max_i16 v176, v176, 0\n"
    "v_pk_max_i16 v177, v177, 0\n"
    "v_pk_max_i16 v178, v178, 0\n"
    "v_pk_max_i16 v179, v179, 0\n"
    "v_cvt_pk_bf16_f32 v184, v184, v185\n"
    "v_cvt_pk_bf16_f32 v185, v186, v187\n"
    "v_cvt_pk_bf16_f32 v186, v188, v189\n"
    "v_mfma_f32_32x32x16_bf16 v[128:143], a[112:115], a[128:131], 0\n"
    "ds_write_b128 v251, v[176:179] offset:27648\n"
    "v_cvt_pk_bf16_f32 v187, v190, v191\n"
    "v_pk_max_i16 v184, v184, 0\n"
    "v_mfma_f32_32x32x16_bf16 v[112:127], v[72:75], v[144:147], v[112:127]\n"
    "s_waitcnt vmcnt(29)\n"
    "v_pk_max_i16 v185, v185, 0\n"
    "v_pk_max_i16 v186, v186, 0\n"
    "v_pk_max_i16 v187, v187, 0\n"
    "v_exp_f32_e32 v152, v152\n"
    "v_exp_f32_e64 v153, v153\n"
    "v_mfma_f32_32x32x16_bf16 v[128:143], a[116:119], a[132:135], v[128:143]\n"
    "ds_read_b128 a[128:131], v243 offset:0\n"
    "v_exp_f32_e32 v154, v154\n"
    "v_exp_f32_e32 v155, v155\n"
    "v_exp_f32_e64 v156, v156\n"
    "v_mfma_f32_32x32x16_bf16 v[128:143], a[120:123], a[136:139], v[128:143]\n"
    "ds_read_b128 a[132:135], v244 offset:0\n"
    "v_exp_f32_e32 v157, v157\n"
    "v_exp_f32_e32 v158, v158\n"
    "v_exp_f32_e64 v159, v159\n"
    "v_mfma_f32_32x32x16_bf16 v[128:143], a[124:127], a[140:143], v[128:143]\n"
    "ds_read_b128 a[136:139], v245 offset:0\n"
    "v_cvt_pk_bf16_f32 v148, v152, v153\n"
    "v_cvt_pk_bf16_f32 v149, v154, v155\n"
    "v_cvt_pk_bf16_f32 v150, v156, v157\n"
    "v_cvt_pk_bf16_f32 v151, v158, v159\n"
    "v_exp_f32_e64 v96, v96\n"
    "v_mfma_f32_32x32x16_bf16 v[192:207], a[24:27], a[152:155], v[192:207]\n"
    "ds_read_b128 a[140:143], v246 offset:0\n"
    "v_exp_f32_e32 v97, v97\n"
    "v_exp_f32_e32 v98, v98\n"
    "v_exp_f32_e64 v99, v99\n"
    "v_mfma_f32_32x32x16_bf16 v[112:127], v[76:79], v[148:151], v[112:127]\n"
    "ds_write_b128 v251, v[184:187] offset:28672\n"
    "v_exp_f32_e64 v100, v100\n"
    "v_smfmac_f32_16x16x64_bf16 v[220:223], v[238:241], v[144:151], v235\n"
    "buffer_store_dwordx4 a[252:255], v242, s[44:47], s97 offen offset:3072 nt\n"
    "v_exp_f32_e64 v101, v101\n"
    "v_mfma_f32_32x32x16_bf16 v[192:207], a[28:31], a[156:159], v[192:207]\n"
    "s_waitcnt lgkmcnt(3)\n"
    "v_exp_f32_e32 v102, v102\n"
    "v_exp_f32_e64 v103, v103\n"
    "v_cvt_pk_bf16_f32 v96, v96, v97\n"
    "v_cvt_pk_bf16_f32 v97, v98, v99\n"
    "v_cvt_pk_bf16_f32 v98, v100, v101\n"
    "v_cvt_pk_bf16_f32 v99, v102, v103\n"
    "v_exp_f32_e32 v104, v104\n"
    "v_exp_f32_e32 v105, v105\n"
    "v_mfma_f32_32x32x16_bf16 v[112:127], v[80:83], v[96:99], v[112:127]\n"
    "s_waitcnt lgkmcnt(1)\n"
    "v_exp_f32_e32 v106, v106\n"
    "v_exp_f32_e32 v107, v107\n"
    "v_exp_f32_e32 v108, v108\n"
    "v_exp_f32_e32 v109, v109\n"
    "v_exp_f32_e32 v110, v110\n"
    "v_exp_f32_e64 v111, v111\n"
    "v_cvt_pk_bf16_f32 v100, v104, v105\n"
    "v_cvt_pk_bf16_f32 v101, v106, v107\n"
    "v_cvt_pk_bf16_f32 v102, v108, v109\n"
    "v_cvt_pk_bf16_f32 v103, v110, v111\n"
    "v_exp_f32_e32 v160, v160\n"
    "v_exp_f32_e32 v161, v161\n"
    "v_mfma_f32_32x32x16_bf16 v[112:127], v[84:87], v[100:103], v[112:127]\n"
    "v_exp_f32_e32 v162, v162\n"
    "v_exp_f32_e32 v163, v163\n"
    "v_exp_f32_e64 v164, v164\n"
    "v_smfmac_f32_16x16x64_bf16 v[220:223], v[238:241], v[96:103], v235\n"
    "v_exp_f32_e32 v165, v165\n"
    "v_exp_f32_e32 v166, v166\n"
    "v_exp_f32_e64 v167, v167\n"
    "v_cvt_pk_bf16_f32 v160, v160, v161\n"
    "v_cvt_pk_bf16_f32 v161, v162, v163\n"
    "v_cvt_pk_bf16_f32 v162, v164, v165\n"
    "v_cvt_pk_bf16_f32 v163, v166, v167\n"
    "v_cvt_pk_bf16_f32 v208, v192, v193\n"
    "v_cvt_pk_bf16_f32 v209, v194, v195\n"
    "v_mfma_f32_32x32x16_bf16 v[112:127], v[88:91], v[160:163], v[112:127]\n"
    "v_cvt_pk_bf16_f32 v210, v196, v197\n"
    "v_cvt_pk_bf16_f32 v211, v198, v199\n"
    "v_cvt_pk_bf16_f32 v212, v200, v201\n"
    "v_cvt_pk_bf16_f32 v213, v202, v203\n"
    "v_cvt_pk_bf16_f32 v214, v204, v205\n"
    "v_cvt_pk_bf16_f32 v215, v206, v207\n"
    "v_mfma_f32_32x32x16_bf16 v[96:111], v[24:27], v[208:211], 0\n"
    "v_exp_f32_e32 v168, v168\n"
    "v_exp_f32_e32 v169, v169\n"
    "v_exp_f32_e64 v170, v170\n"
    "v_mfma_f32_32x32x16_bf16 v[96:111], v[28:31], v[212:215], v[96:111]\n"
    "v_exp_f32_e32 v171, v171\n"
    "v_exp_f32_e32 v172, v172\n"
    "v_exp_f32_e64 v173, v173\n"
    "v_mfma_f32_16x16x32_bf16 v[216:219], a[184:187], v[208:211], 0\n"
    "v_exp_f32_e64 v174, v174\n"
    "v_mfma_f32_16x16x32_bf16 v[216:219], a[188:191], v[212:215], v[216:219]\n"
    "v_exp_f32_e64 v175, v175\n"
    "v_cvt_pk_bf16_f32 v164, v168, v169\n"
    "v_cvt_pk_bf16_f32 v165, v170, v171\n"
    "v_cvt_pk_bf16_f32 v166, v172, v173\n"
    "v_cvt_pk_bf16_f32 v167, v174, v175\n"
    "s_nop 1\n"
    "v_mfma_f32_32x32x16_bf16 v[112:127], v[92:95], v[164:167], v[112:127]\n"
    "v_smfmac_f32_16x16x64_bf16 v[220:223], v[238:241], v[160:167], v235\n"
    "s_waitcnt lgkmcnt(0)\n"
    "s_add_u32 s44, s44, s39\n"
    "s_addc_u32 s45, s45, 0\n"
    "s_cmp_lt_i32 s59, s36\n"
    "s_cbranch_scc1 .Lloop1_%=\n"
    ".p2align 3\n"
    ".Llast_%=:\n"
    "s_nop 5\n"
    "v_mfma_f32_32x32x16_bf16 v[144:159], v[32:35], v[208:211], 0\n"
    "v_add_f32_e32 v236, v220, v221\n"
    "v_exp_f32_e32 v216, v216\n"
    "v_rcp_f32_e32 v232, v236\n"
    "v_exp_f32_e64 v217, v217\n"
    "v_mfma_f32_32x32x16_bf16 v[144:159], v[36:39], v[212:215], v[144:159]\n"
    "v_maximum3_f32 v233, v233, v236, v232\n"
    "v_cvt_pk_bf16_f32 v230, v216, v217\n"
    "v_exp_f32_e32 v96, v96\n"
    "v_exp_f32_e32 v97, v97\n"
    "v_exp_f32_e32 v98, v98\n"
    "v_exp_f32_e32 v99, v99\n"
    "v_pk_fma_f32 v[128:129], v[112:113], v[232:233], v[128:129] op_sel_hi:[1,0,1]\n"
    "v_pk_fma_f32 v[130:131], v[114:115], v[232:233], v[130:131] op_sel_hi:[1,0,1]\n"
    "v_pk_fma_f32 v[132:133], v[116:117], v[232:233], v[132:133] op_sel_hi:[1,0,1]\n"
    "v_pk_fma_f32 v[134:135], v[118:119], v[232:233], v[134:135] op_sel_hi:[1,0,1]\n"
    "v_pk_fma_f32 v[136:137], v[120:121], v[232:233], v[136:137] op_sel_hi:[1,0,1]\n"
    "v_pk_fma_f32 v[138:139], v[122:123], v[232:233], v[138:139] op_sel_hi:[1,0,1]\n"
    "v_mfma_f32_16x16x16_bf16 v[220:223], v[234:235], v[230:231], 0\n"
    "v_exp_f32_e32 v100, v100\n"
    "v_exp_f32_e32 v101, v101\n"
    "v_exp_f32_e64 v102, v102\n"
    "v_pk_fma_f32 v[140:141], v[124:125], v[232:233], v[140:141] op_sel_hi:[1,0,1]\n"
    "v_pk_fma_f32 v[142:143], v[126:127], v[232:233], v[142:143] op_sel_hi:[1,0,1]\n"
    "v_mfma_f32_32x32x8_bf16 v[160:175], v[226:227], v[230:231], 0\n"
    "v_exp_f32_e64 v103, v103\n"
    "v_cvt_pk_bf16_f32 v96, v96, v97\n"
    "v_cvt_pk_bf16_f32 v97, v98, v99\n"
    "v_cvt_pk_bf16_f32 v98, v100, v101\n"
    "v_cvt_pk_bf16_f32 v99, v102, v103\n"
    "v_exp_f32_e64 v104, v104\n"
    "v_mfma_f32_32x32x16_bf16 v[176:191], a[112:115], a[144:147], 0\n"
    "v_exp_f32_e32 v105, v105\n"
    "v_exp_f32_e32 v106, v106\n"
    "v_exp_f32_e64 v107, v107\n"
    "v_mfma_f32_32x32x16_bf16 v[160:175], v[72:75], v[96:99], v[160:175]\n"
    "v_exp_f32_e32 v108, v108\n"
    "v_exp_f32_e32 v109, v109\n"
    "v_exp_f32_e64 v110, v110\n"
    "v_mfma_f32_32x32x16_bf16 v[176:191], a[116:119], a[148:151], v[176:191]\n"
    "v_exp_f32_e64 v111, v111\n"
    "v_cvt_pk_bf16_f32 v100, v104, v105\n"
    "v_cvt_pk_bf16_f32 v101, v106, v107\n"
    "v_cvt_pk_bf16_f32 v102, v108, v109\n"
    "v_cvt_pk_bf16_f32 v103, v110, v111\n"
    "v_mfma_f32_32x32x16_bf16 v[176:191], a[120:123], a[152:155], v[176:191]\n"
    "v_cvt_pk_bf16_f32 v128, v128, v129\n"
    "v_cvt_pk_bf16_f32 v129, v130, v131\n"
    "v_cvt_pk_bf16_f32 v130, v132, v133\n"
    "v_cvt_pk_bf16_f32 v131, v134, v135\n"
    "v_pk_max_i16 v128, v128, 0\n"
    "v_pk_max_i16 v129, v129, 0\n"
    "v_mfma_f32_32x32x16_bf16 v[160:175], v[76:79], v[100:103], v[160:175]\n"
    "v_pk_max_i16 v130, v130, 0\n"
    "v_pk_max_i16 v131, v131, 0\n"
    "v_cvt_pk_bf16_f32 v136, v136, v137\n"
    "v_pk_max_i16 v136, v136, 0\n"
    "v_cvt_pk_bf16_f32 v137, v138, v139\n"
    "v_pk_max_i16 v137, v137, 0\n"
    "v_smfmac_f32_16x16x64_bf16 v[220:223], v[238:241], v[96:103], v235\n"
    "ds_write_b128 v251, v[128:131] offset:21504\n"
    "v_mfma_f32_32x32x16_bf16 v[176:191], a[124:127], a[156:159], v[176:191]\n"
    "v_cvt_pk_bf16_f32 v138, v140, v141\n"
    "v_pk_max_i16 v138, v138, 0\n"
    "v_cvt_pk_bf16_f32 v139, v142, v143\n"
    "v_pk_max_i16 v139, v139, 0\n"
    "v_exp_f32_e64 v144, v144\n"
    "v_mfma_f32_32x32x16_bf16 v[192:207], v[40:43], v[208:211], 0\n"
    "ds_write_b128 v251, v[136:139] offset:22528\n"
    "v_exp_f32_e64 v145, v145\n"
    "v_mfma_f32_32x32x16_bf16 v[192:207], v[44:47], v[212:215], v[192:207]\n"
    "ds_read_b128 a[192:195], v254 offset:21504\n"
    "v_exp_f32_e32 v146, v146\n"
    "v_exp_f32_e32 v147, v147\n"
    "v_exp_f32_e64 v148, v148\n"
    "v_mfma_f32_32x32x16_bf16 v[112:127], a[16:19], a[160:163], 0\n"
    "v_exp_f32_e32 v149, v149\n"
    "v_exp_f32_e32 v150, v150\n"
    "v_exp_f32_e64 v151, v151\n"
    "v_mfma_f32_32x32x16_bf16 v[112:127], a[20:23], a[164:167], v[112:127]\n"
    "v_cvt_pk_bf16_f32 v144, v144, v145\n"
    "v_cvt_pk_bf16_f32 v145, v146, v147\n"
    "v_cvt_pk_bf16_f32 v146, v148, v149\n"
    "v_cvt_pk_bf16_f32 v147, v150, v151\n"
    "v_exp_f32_e64 v152, v152\n"
    "v_mfma_f32_32x32x16_bf16 v[112:127], a[24:27], a[168:171], v[112:127]\n"
    "ds_read_b128 a[196:199], v254 offset:21632\n"
    "v_exp_f32_e32 v153, v153\n"
    "v_exp_f32_e32 v154, v154\n"
    "v_exp_f32_e64 v155, v155\n"
    "v_mfma_f32_32x32x16_bf16 v[160:175], v[80:83], v[144:147], v[160:175]\n"
    "v_exp_f32_e32 v156, v156\n"
    "v_exp_f32_e32 v157, v157\n"
    "v_exp_f32_e64 v158, v158\n"
    "v_mfma_f32_32x32x16_bf16 v[112:127], a[28:31], a[172:175], v[112:127]\n"
    "s_waitcnt lgkmcnt(1)\n"
    "buffer_store_dwordx4 a[192:195], v242, s[44:47], 0 offen offset:0 nt\n"
    "v_exp_f32_e32 v159, v159\n"
    "v_cvt_pk_bf16_f32 v148, v152, v153\n"
    "v_cvt_pk_bf16_f32 v149, v154, v155\n"
    "v_cvt_pk_bf16_f32 v150, v156, v157\n"
    "v_cvt_pk_bf16_f32 v151, v158, v159\n"
    "v_exp_f32_e32 v192, v192\n"
    "v_exp_f32_e32 v193, v193\n"
    "v_mfma_f32_32x32x16_bf16 v[160:175], v[84:87], v[148:151], v[160:175]\n"
    "v_exp_f32_e32 v194, v194\n"
    "v_exp_f32_e32 v195, v195\n"
    "v_cvt_pk_bf16_f32 v208, v112, v113\n"
    "v_cvt_pk_bf16_f32 v209, v114, v115\n"
    "v_smfmac_f32_16x16x64_bf16 v[220:223], v[238:241], v[144:151], v235\n"
    "s_waitcnt lgkmcnt(0)\n"
    "buffer_store_dwordx4 a[196:199], v242, s[44:47], 0 offen offset:1024 nt\n"
    "v_cvt_pk_bf16_f32 v210, v116, v117\n"
    "v_cvt_pk_bf16_f32 v211, v118, v119\n"
    "v_cvt_pk_bf16_f32 v212, v120, v121\n"
    "v_cvt_pk_bf16_f32 v213, v122, v123\n"
    "v_mfma_f32_32x32x16_bf16 v[96:111], v[32:35], v[208:211], 0\n"
    "v_cvt_pk_bf16_f32 v214, v124, v125\n"
    "v_cvt_pk_bf16_f32 v215, v126, v127\n"
    "v_exp_f32_e32 v196, v196\n"
    "v_exp_f32_e64 v197, v197\n"
    "v_mfma_f32_32x32x16_bf16 v[96:111], v[36:39], v[212:215], v[96:111]\n"
    "v_exp_f32_e32 v198, v198\n"
    "v_exp_f32_e32 v199, v199\n"
    "v_cvt_pk_bf16_f32 v192, v192, v193\n"
    "v_exp_f32_e64 v200, v200\n"
    "v_mfma_f32_32x32x16_bf16 v[128:143], v[24:27], v[208:211], 0\n"
    "ds_read_b128 a[200:203], v254 offset:21760\n"
    "v_cvt_pk_bf16_f32 v193, v194, v195\n"
    "v_exp_f32_e64 v201, v201\n"
    "v_cvt_pk_bf16_f32 v194, v196, v197\n"
    "v_exp_f32_e64 v202, v202\n"
    "v_mfma_f32_32x32x16_bf16 v[128:143], v[28:31], v[212:215], v[128:143]\n"
    "v_cvt_pk_bf16_f32 v195, v198, v199\n"
    "v_exp_f32_e32 v203, v203\n"
    "v_exp_f32_e32 v204, v204\n"
    "v_exp_f32_e64 v205, v205\n"
    "v_mfma_f32_32x32x16_bf16 v[160:175], v[88:91], v[192:195], v[160:175]\n"
    "v_exp_f32_e32 v206, v206\n"
    "v_exp_f32_e32 v207, v207\n"
    "v_cvt_pk_bf16_f32 v196, v200, v201\n"
    "v_cvt_pk_bf16_f32 v197, v202, v203\n"
    "v_mfma_f32_16x16x32_bf16 v[216:219], a[184:187], v[208:211], 0\n"
    "v_cvt_pk_bf16_f32 v198, v204, v205\n"
    "v_cvt_pk_bf16_f32 v199, v206, v207\n"
    "v_mfma_f32_16x16x32_bf16 v[216:219], a[188:191], v[212:215], v[216:219]\n"
    "v_exp_f32_e64 v128, v128\n"
    "v_mfma_f32_32x32x16_bf16 v[160:175], v[92:95], v[196:199], v[160:175]\n"
    "v_exp_f32_e32 v129, v129\n"
    "v_exp_f32_e32 v130, v130\n"
    "v_exp_f32_e64 v131, v131\n"
    "v_smfmac_f32_16x16x64_bf16 v[220:223], v[238:241], v[192:199], v235\n"
    "s_waitcnt lgkmcnt(0)\n"
    "buffer_store_dwordx4 a[200:203], v242, s[44:47], 0 offen offset:2048 nt\n"
    "v_mfma_f32_32x32x16_bf16 v[112:127], a[112:115], a[160:163], 0\n"
    "v_exp_f32_e32 v216, v216\n"
    "v_exp_f32_e32 v217, v217\n"
    "v_exp_f32_e32 v132, v132\n"
    "v_mfma_f32_32x32x16_bf16 v[112:127], a[116:119], a[164:167], v[112:127]\n"
    "v_cvt_pk_bf16_f32 v230, v216, v217\n"
    "v_add_f32_e32 v236, v220, v221\n"
    "v_rcp_f32_e32 v232, v236\n"
    "v_exp_f32_e64 v133, v133\n"
    "v_mfma_f32_16x16x16_bf16 v[220:223], v[234:235], v[230:231], 0\n"
    "v_maximum3_f32 v233, v233, v236, v232\n"
    "v_exp_f32_e32 v134, v134\n"
    "v_exp_f32_e32 v135, v135\n"
    "v_cvt_pk_bf16_f32 v128, v128, v129\n"
    "v_pk_fma_f32 v[176:177], v[160:161], v[232:233], v[176:177] op_sel_hi:[1,0,1]\n"
    "v_pk_fma_f32 v[178:179], v[162:163], v[232:233], v[178:179] op_sel_hi:[1,0,1]\n"
    "v_pk_fma_f32 v[180:181], v[164:165], v[232:233], v[180:181] op_sel_hi:[1,0,1]\n"
    "v_pk_fma_f32 v[182:183], v[166:167], v[232:233], v[182:183] op_sel_hi:[1,0,1]\n"
    "v_pk_fma_f32 v[184:185], v[168:169], v[232:233], v[184:185] op_sel_hi:[1,0,1]\n"
    "v_pk_fma_f32 v[186:187], v[170:171], v[232:233], v[186:187] op_sel_hi:[1,0,1]\n"
    "v_pk_fma_f32 v[188:189], v[172:173], v[232:233], v[188:189] op_sel_hi:[1,0,1]\n"
    "v_pk_fma_f32 v[190:191], v[174:175], v[232:233], v[190:191] op_sel_hi:[1,0,1]\n"
    "v_mfma_f32_32x32x8_bf16 v[144:159], v[226:227], v[230:231], 0\n"
    "v_cvt_pk_bf16_f32 v129, v130, v131\n"
    "v_cvt_pk_bf16_f32 v130, v132, v133\n"
    "v_cvt_pk_bf16_f32 v131, v134, v135\n"
    "v_cvt_pk_bf16_f32 v176, v176, v177\n"
    "v_cvt_pk_bf16_f32 v177, v178, v179\n"
    "v_cvt_pk_bf16_f32 v178, v180, v181\n"
    "v_cvt_pk_bf16_f32 v179, v182, v183\n"
    "v_mfma_f32_32x32x16_bf16 v[112:127], a[120:123], a[168:171], v[112:127]\n"
    "ds_read_b128 a[204:207], v254 offset:21888\n"
    "v_pk_max_i16 v176, v176, 0\n"
    "v_pk_max_i16 v177, v177, 0\n"
    "v_pk_max_i16 v178, v178, 0\n"
    "v_pk_max_i16 v179, v179, 0\n"
    "v_cvt_pk_bf16_f32 v184, v184, v185\n"
    "v_mfma_f32_32x32x16_bf16 v[144:159], v[72:75], v[128:131], v[144:159]\n"
    "ds_write_b128 v251, v[176:179] offset:23552\n"
    "v_pk_max_i16 v184, v184, 0\n"
    "v_cvt_pk_bf16_f32 v185, v186, v187\n"
    "v_mfma_f32_32x32x16_bf16 v[112:127], a[124:127], a[172:175], v[112:127]\n"
    "v_pk_max_i16 v185, v185, 0\n"
    "v_cvt_pk_bf16_f32 v186, v188, v189\n"
    "v_pk_max_i16 v186, v186, 0\n"
    "v_cvt_pk_bf16_f32 v187, v190, v191\n"
    "v_pk_max_i16 v187, v187, 0\n"
    "v_exp_f32_e64 v104, v104\n"
    "v_mfma_f32_32x32x16_bf16 v[192:207], v[40:43], v[208:211], 0\n"
    "ds_write_b128 v251, v[184:187] offset:24576\n"
    "v_exp_f32_e64 v105, v105\n"
    "v_mfma_f32_32x32x16_bf16 v[192:207], v[44:47], v[212:215], v[192:207]\n"
    "ds_read_b128 a[208:211], v254 offset:23552\n"
    "v_exp_f32_e32 v106, v106\n"
    "v_exp_f32_e32 v107, v107\n"
    "v_exp_f32_e32 v108, v108\n"
    "v_exp_f32_e32 v109, v109\n"
    "v_exp_f32_e32 v110, v110\n"
    "v_exp_f32_e32 v136, v136\n"
    "v_exp_f32_e32 v111, v111\n"
    "v_exp_f32_e32 v137, v137\n"
    "v_exp_f32_e32 v138, v138\n"
    "v_exp_f32_e32 v139, v139\n"
    "v_exp_f32_e32 v140, v140\n"
    "v_exp_f32_e32 v141, v141\n"
    "v_exp_f32_e32 v142, v142\n"
    "v_exp_f32_e32 v143, v143\n"
    "v_cvt_pk_bf16_f32 v132, v136, v137\n"
    "v_cvt_pk_bf16_f32 v133, v138, v139\n"
    "v_cvt_pk_bf16_f32 v134, v140, v141\n"
    "v_cvt_pk_bf16_f32 v135, v142, v143\n"
    "v_exp_f32_e32 v96, v96\n"
    "v_exp_f32_e32 v97, v97\n"
    "v_mfma_f32_32x32x16_bf16 v[144:159], v[76:79], v[132:135], v[144:159]\n"
    "s_waitcnt lgkmcnt(0)\n"
    "buffer_store_dwordx4 a[204:207], v242, s[44:47], 0 offen offset:3072 nt\n"
    "v_exp_f32_e32 v98, v98\n"
    "v_exp_f32_e64 v99, v99\n"
    "v_smfmac_f32_16x16x64_bf16 v[220:223], v[238:241], v[128:135], v235\n"
    "buffer_store_dwordx4 a[208:211], v242, s[44:47], s96 offen offset:0 nt\n"
    "v_exp_f32_e32 v192, v192\n"
    "v_exp_f32_e32 v100, v100\n"
    "v_exp_f32_e32 v193, v193\n"
    "v_exp_f32_e32 v101, v101\n"
    "v_exp_f32_e32 v194, v194\n"
    "v_exp_f32_e32 v102, v102\n"
    "v_exp_f32_e32 v195, v195\n"
    "v_exp_f32_e32 v103, v103\n"
    "v_exp_f32_e64 v196, v196\n"
    "v_cvt_pk_bf16_f32 v96, v96, v97\n"
    "v_exp_f32_e64 v197, v197\n"
    "v_cvt_pk_bf16_f32 v97, v98, v99\n"
    "v_exp_f32_e64 v198, v198\n"
    "v_cvt_pk_bf16_f32 v98, v100, v101\n"
    "v_cvt_pk_bf16_f32 v100, v104, v105\n"
    "v_cvt_pk_bf16_f32 v101, v106, v107\n"
    "v_exp_f32_e64 v199, v199\n"
    "v_cvt_pk_bf16_f32 v99, v102, v103\n"
    "v_cvt_pk_bf16_f32 v102, v108, v109\n"
    "v_cvt_pk_bf16_f32 v103, v110, v111\n"
    "v_mfma_f32_32x32x16_bf16 v[144:159], v[80:83], v[96:99], v[144:159]\n"
    "ds_read_b128 a[212:215], v254 offset:23680\n"
    "v_cvt_pk_bf16_f32 v192, v192, v193\n"
    "v_cvt_pk_bf16_f32 v193, v194, v195\n"
    "v_cvt_pk_bf16_f32 v194, v196, v197\n"
    "v_cvt_pk_bf16_f32 v195, v198, v199\n"
    "v_exp_f32_e64 v200, v200\n"
    "v_mfma_f32_32x32x16_bf16 v[144:159], v[84:87], v[100:103], v[144:159]\n"
    "v_exp_f32_e32 v201, v201\n"
    "v_exp_f32_e32 v202, v202\n"
    "v_exp_f32_e64 v203, v203\n"
    "v_smfmac_f32_16x16x64_bf16 v[220:223], v[238:241], v[96:103], v235\n"
    "v_exp_f32_e64 v204, v204\n"
    "v_mfma_f32_32x32x16_bf16 v[144:159], v[88:91], v[192:195], v[144:159]\n"
    "v_exp_f32_e32 v205, v205\n"
    "v_exp_f32_e32 v206, v206\n"
    "v_exp_f32_e64 v207, v207\n"
    "v_cvt_pk_bf16_f32 v196, v200, v201\n"
    "v_cvt_pk_bf16_f32 v197, v202, v203\n"
    "v_cvt_pk_bf16_f32 v198, v204, v205\n"
    "v_cvt_pk_bf16_f32 v199, v206, v207\n"
    "ds_read_b128 a[216:219], v254 offset:23808\n"
    "ds_read_b128 a[220:223], v254 offset:23936\n"
    "v_mfma_f32_32x32x16_bf16 v[144:159], v[92:95], v[196:199], v[144:159]\n"
    "s_waitcnt lgkmcnt(2)\n"
    "buffer_store_dwordx4 a[212:215], v242, s[44:47], s96 offen offset:1024 nt\n"
    "v_smfmac_f32_16x16x64_bf16 v[220:223], v[238:241], v[192:199], v235\n"
    "s_nop 9\n"
    "v_add_f32_e32 v236, v220, v221\n"
    "v_rcp_f32_e64 v232, v236\n"
    "s_nop 0\n"
    "v_pk_fma_f32 v[112:113], v[144:145], v[232:233], v[112:113] op_sel_hi:[1,0,1]\n"
    "v_pk_fma_f32 v[114:115], v[146:147], v[232:233], v[114:115] op_sel_hi:[1,0,1]\n"
    "v_pk_fma_f32 v[116:117], v[148:149], v[232:233], v[116:117] op_sel_hi:[1,0,1]\n"
    "v_pk_fma_f32 v[118:119], v[150:151], v[232:233], v[118:119] op_sel_hi:[1,0,1]\n"
    "v_pk_fma_f32 v[120:121], v[152:153], v[232:233], v[120:121] op_sel_hi:[1,0,1]\n"
    "v_pk_fma_f32 v[122:123], v[154:155], v[232:233], v[122:123] op_sel_hi:[1,0,1]\n"
    "v_pk_fma_f32 v[124:125], v[156:157], v[232:233], v[124:125] op_sel_hi:[1,0,1]\n"
    "v_pk_fma_f32 v[126:127], v[158:159], v[232:233], v[126:127] op_sel_hi:[1,0,1]\n"
    "v_maximum3_f32 v233, v233, v236, v232\n"
    "v_cvt_pk_bf16_f32 v112, v112, v113\n"
    "v_cvt_pk_bf16_f32 v113, v114, v115\n"
    "v_cvt_pk_bf16_f32 v114, v116, v117\n"
    "v_cvt_pk_bf16_f32 v115, v118, v119\n"
    "v_pk_max_i16 v112, v112, 0\n"
    "v_pk_max_i16 v113, v113, 0\n"
    "v_pk_max_i16 v114, v114, 0\n"
    "v_pk_max_i16 v115, v115, 0\n"
    "v_cvt_pk_bf16_f32 v120, v120, v121\n"
    "v_cvt_pk_bf16_f32 v121, v122, v123\n"
    "v_pk_max_i16 v121, v121, 0\n"
    "v_cvt_pk_bf16_f32 v122, v124, v125\n"
    "v_pk_max_i16 v122, v122, 0\n"
    "v_cvt_pk_bf16_f32 v123, v126, v127\n"
    "v_pk_max_i16 v123, v123, 0\n"
    "v_pk_max_i16 v120, v120, 0\n"
    "ds_write_b128 v251, v[112:115] offset:25600\n"
    "ds_write_b128 v251, v[120:123] offset:26624\n"
    "ds_read_b128 a[224:227], v254 offset:25600\n"
    "ds_read_b128 a[228:231], v254 offset:25728\n"
    "ds_read_b128 a[232:235], v254 offset:25856\n"
    "ds_read_b128 a[236:239], v254 offset:25984\n"
    "s_waitcnt lgkmcnt(6)\n"
    "buffer_store_dwordx4 a[216:219], v242, s[44:47], s96 offen offset:2048 nt\n"
    "buffer_store_dwordx4 a[220:223], v242, s[44:47], s96 offen offset:3072 nt\n"
    "s_waitcnt lgkmcnt(3)\n"
    "buffer_store_dwordx4 a[224:227], v242, s[44:47], s97 offen offset:0 nt\n"
    "s_waitcnt lgkmcnt(2)\n"
    "buffer_store_dwordx4 a[228:231], v242, s[44:47], s97 offen offset:1024 nt\n"
    "s_waitcnt lgkmcnt(0)\n"
    "buffer_store_dwordx4 a[232:235], v242, s[44:47], s97 offen offset:2048 nt\n"
    "buffer_store_dwordx4 a[236:239], v242, s[44:47], s97 offen offset:3072 nt\n"
    "s_waitcnt lgkmcnt(0)\n"
    "s_waitcnt vmcnt(12)\n"
    "s_barrier\n"
    : "+{s37}"(bb), "+{s[40:41]}"(nsrc), "+{s[42:43]}"(dst), "={v233}"(chk), "+{v232}"(ta1), "+{v236}"(ta0), "+{v230}"(ta2), "+{v228}"(pw), "+{v220}"(pe), "+{v222}"(pe1), "+{v221}"(pv), "+{v229}"(vadr), "+{v247}"(lh0), "+{v248}"(lh1), "+{v249}"(lk), "+{v250}"(lw), "+{v243}"(ea0), "+{v244}"(ea1), "+{v245}"(ea2), "+{v246}"(ea3)
    : "{s35}"(alt), "{s36}"(bend), "{s38}"(nw), "{s39}"(step), "{s52}"(ebase),
      "{v237}"(pk), "{v234}"(sel1), "{v235}"(sidx), "{v238}"(sela), "{v239}"(sela), "{v240}"(selb), "{v241}"(selb), "{v242}"(wadr),
      "{v251}"(sadr), "{v252}"(voff_e), "{v253}"(voff_o), "{v254}"(radr)
    : "v0", "v1", "v2", "v3", "v4", "v5", "v6", "v7", "v8", "v9", "v10", "v11", "v12", "v13", "v14", "v15", "v16", "v17", "v18", "v19", "v20", "v21", "v22", "v23", "v24", "v25", "v26", "v27", "v28", "v29", "v30", "v31", "v32", "v33", "v34", "v35", "v36", "v37", "v38", "v39", "v40", "v41", "v42", "v43", "v44", "v45", "v46", "v47", "v48", "v49", "v50", "v51", "v52", "v53", "v54", "v55", "v56", "v57", "v58", "v59", "v60", "v61", "v62", "v63", "v64", "v65", "v66", "v67", "v68", "v69", "v70", "v71", "v72", "v73", "v74", "v75", "v76", "v77", "v78", "v79", "v80", "v81", "v82", "v83", "v84", "v85", "v86", "v87", "v88", "v89", "v90", "v91", "v92", "v93", "v94", "v95", "v96", "v97", "v98", "v99", "v100", "v101", "v102", "v103", "v104", "v105", "v106", "v107", "v108", "v109", "v110", "v111", "v112", "v113", "v114", "v115", "v116", "v117", "v118", "v119", "v120", "v121", "v122", "v123", "v124", "v125", "v126", "v127", "v128", "v129", "v130", "v131", "v132", "v133", "v134", "v135", "v136", "v137", "v138", "v139", "v140", "v141", "v142", "v143", "v144", "v145", "v146", "v147", "v148", "v149", "v150", "v151", "v152", "v153", "v154", "v155", "v156", "v157", "v158", "v159", "v160", "v161", "v162", "v163", "v164", "v165", "v166", "v167", "v168", "v169", "v170", "v171", "v172", "v173", "v174", "v175", "v176", "v177", "v178", "v179", "v180", "v181", "v182", "v183", "v184", "v185", "v186", "v187", "v188", "v189", "v190", "v191", "v192", "v193", "v194", "v195", "v196", "v197", "v198", "v199", "v200", "v201", "v202", "v203", "v204", "v205", "v206", "v207", "v208", "v209", "v210", "v211", "v212", "v213", "v214", "v215", "v216", "v217", "v218", "v219", "v223", "v224", "v225", "v226", "v227", "v231", "a0", "a1", "a2", "a3", "a4", "a5", "a6", "a7", "a8", "a9", "a10", "a11", "a12", "a13", "a14", "a15", "a16", "a17", "a18", "a19", "a20", "a21", "a22", "a23", "a24", "a25", "a26", "a27", "a28", "a29", "a30", "a31", "a32", "a33", "a34", "a35", "a36", "a37", "a38", "a39", "a40", "a41", "a42", "a43", "a44", "a45", "a46", "a47", "a48", "a49", "a50", "a51", "a52", "a53", "a54", "a55", "a56", "a57", "a58", "a59", "a60", "a61", "a62", "a63", "a64", "a65", "a66", "a67", "a68", "a69", "a70", "a71", "a72", "a73", "a74", "a75", "a76", "a77", "a78", "a79", "a80", "a81", "a82", "a83", "a84", "a85", "a86", "a87", "a88", "a89", "a90", "a91", "a92", "a93", "a94", "a95", "a96", "a97", "a98", "a99", "a100", "a101", "a102", "a103", "a104", "a105", "a106", "a107", "a108", "a109", "a110", "a111", "a112", "a113", "a114", "a115", "a116", "a117", "a118", "a119", "a120", "a121", "a122", "a123", "a124", "a125", "a126", "a127", "a128", "a129", "a130", "a131", "a132", "a133", "a134", "a135", "a136", "a137", "a138", "a139", "a140", "a141", "a142", "a143", "a144", "a145", "a146", "a147", "a148", "a149", "a150", "a151", "a152", "a153", "a154", "a155", "a156", "a157", "a158", "a159", "a160", "a161", "a162", "a163", "a164", "a165", "a166", "a167", "a168", "a169", "a170", "a171", "a172", "a173", "a174", "a175", "a176", "a177", "a178", "a179", "a180", "a181", "a182", "a183", "a184", "a185", "a186", "a187", "a188", "a189", "a190", "a191", "a192", "a193", "a194", "a195", "a196", "a197", "a198", "a199", "a200", "a201", "a202", "a203", "a204", "a205", "a206", "a207", "a208", "a209", "a210", "a211", "a212", "a213", "a214", "a215", "a216", "a217", "a218", "a219", "a220", "a221", "a222", "a223", "a224", "a225", "a226", "a227", "a228", "a229", "a230", "a231", "a232", "a233", "a234", "a235", "a236", "a237", "a238", "a239", "a240", "a241", "a242", "a243", "a244", "a245", "a246", "a247", "a248", "a249", "a250", "a251", "a252", "a253", "a254", "a255", "s34", "s86", "s87", "s88", "s89", "s90", "s91", "s92", "s93", "s94", "s95", "s96", "s97", "s98", "s99", "s44", "s45", "s46", "s47", "s48", "s49", "s50", "s51", "s53", "s54", "s55", "s56", "s57", "s58", "s59", "s60", "s61", "s62", "s63", "scc", "vcc", "memory");
  if (__builtin_expect(__any(!(chk <= CHK_LIMIT)), 0)) {
    int l2 = (int)__builtin_amdgcn_mbcnt_hi(~0u, __builtin_amdgcn_mbcnt_lo(~0u, 0u));
    asm volatile("s_waitcnt vmcnt(0)" : "+v"(l2) :: "memory");
    const int r2 = l2 & 31, hf2 = l2 >> 5;
    asm volatile(
      "ds_write_b128 %0, a[0:3] offset:0\n"
      "ds_write_b128 %0, a[4:7] offset:1024\n"
      "ds_write_b128 %0, a[8:11] offset:2048\n"
      "ds_write_b128 %0, a[12:15] offset:3072\n"
      "ds_write_b128 %0, a[16:19] offset:4096\n"
      "ds_write_b128 %0, a[20:23] offset:5120\n"
      "ds_write_b128 %0, a[24:27] offset:6144\n"
      "ds_write_b128 %0, a[28:31] offset:7168\n"
      "ds_write_b128 %0, a[32:35] offset:8192\n"
      "ds_write_b128 %0, a[36:39] offset:9216\n"
      "ds_write_b128 %0, a[40:43] offset:10240\n"
      "ds_write_b128 %0, a[44:47] offset:11264\n"
      "ds_write_b128 %0, a[48:51] offset:12288\n"
      "ds_write_b128 %0, a[52:55] offset:13312\n"
      "ds_write_b128 %0, a[56:59] offset:14336\n"
      "ds_write_b128 %0, a[60:63] offset:15360\n"
      "ds_write_b128 %0, a[64:67] offset:16384\n"
      "ds_write_b128 %0, a[68:71] offset:17408\n"
      "ds_write_b128 %0, a[72:75] offset:18432\n"
      "ds_write_b128 %0, a[76:79] offset:19456\n"
      "ds_write_b128 %0, a[80:83] offset:20480\n"
      "ds_write_b128 %0, a[84:87] offset:21504\n"
      "ds_write_b128 %0, a[88:91] offset:22528\n"
      "ds_write_b128 %0, a[92:95] offset:23552\n"
      "ds_write_b128 %0, a[96:99] offset:24576\n"
      "ds_write_b128 %0, a[100:103] offset:25600\n"
      "ds_write_b128 %0, a[104:107] offset:26624\n"
      "ds_write_b128 %0, a[108:111] offset:27648\n"
      "ds_write_b128 %0, a[112:115] offset:28672\n"
      "ds_write_b128 %0, a[116:119] offset:29696\n"
      "ds_write_b128 %0, a[120:123] offset:30720\n"
      "ds_write_b128 %0, a[124:127] offset:31744\n"
      "s_waitcnt lgkmcnt(0)" :: "v"(wadr) : "memory");
    load_first<0>(src0, wadr, voff_e, voff_o, ebase); load_rest(src0, voff_e, voff_o, ebase);
    {
      const int G2 = l2 >> 4, c2 = l2 & 15, ch2 = c2 >> 3, ct2 = (c2 >> 1) & 3, cs2 = c2 & 1;
      const bool cv2 = (G2 & 1) == cs2, kv2 = ((c2 & 2) == 0) && (((c2 >> 2) & 1) == (G2 & 1));
      ea0 = ebase + r2 * 128 + (((0 + hf2) ^ ((r2 >> 1) & 7)) << 4); ea1 = ea0 ^ 32; ea2 = ea0 ^ 64; ea3 = ea0 ^ 96;
      lh0 = ((cv2 && ch2 == 0) ? lbase + ct2 * 128 : zbase) + hf2 * 16;
      lh1 = ((cv2 && ch2 == 1) ? lbase + ct2 * 128 : zbase) + hf2 * 16;
      lk = (kv2 ? lbase + (2 * (c2 >> 3) + (c2 & 1)) * 128 : zbase) + hf2 * 16;
      lw = lbase + ct2 * 128 + ch2 * 64 + cs2 * 32 + (G2 & 1) * 16 + (G2 >> 1) * 8;
    }
    const unsigned ooff = r2 * 128 + hf2 * 16;
    const unsigned one2 = (((l2 >> 4) & 1) == ((l2 >> 2) & 1)) ? 0x3f803f80u : 0u;
    const unsigned neg0 = hf2 ? 0u : 0xbf80bf80u, neg1 = hf2 ? 0u : 0x0000bf80u;
    nsrc = src0 + step; dst = dst0; bb = b;
    asm volatile(
    "s_mov_b32 s54, 0xaaaa5555\n"
    "s_mov_b32 s55, 0xaaaa5555\n"
    "s_mov_b32 s56, 0x00ff00ff\n"
    "s_mov_b32 s57, 0x00ff00ff\n"
    "s_mov_b32 s58, 0x07060302\n"
    "s_mov_b32 s59, 0xffff0000\n"
    "s_mov_b32 s62, -1\n"
    "s_mov_b32 s63, 0\n"
    "v_mov_b32 v193, 0\n"
    "v_mov_b32 v195, 0\n"
    "v_mov_b32 v197, 0\n"
    "v_mov_b32 v202, 0xff800000\n"
    "s_add_u32 s53, s52, 12288\n"
    ".p2align 6\n"
    ".Lloop_%=:\n"
    "s_add_i32 s37, s37, s38\n"
    "s_add_u32 s44, s42, 4096\n"
    "s_addc_u32 s45, s43, 0\n"
    "s_add_u32 s46, s42, 12288\n"
    "s_addc_u32 s47, s43, 0\n"
    "s_sub_u32 s48, s46, s53\n"
    "s_subb_u32 s49, s47, 0\n"
    "s_nop 0\n"
    "ds_read_b128 v[72:75], v242 offset:8192\n"
    "ds_read_b128 v[76:79], v242 offset:9216\n"
    "ds_read_b128 v[80:83], v242 offset:10240\n"
    "ds_read_b128 v[84:87], v242 offset:11264\n"
    "ds_read_b128 v[212:215], v242 offset:12288\n"
    "ds_read_b128 v[216:219], v242 offset:13312\n"
    "ds_read_b128 v[220:223], v242 offset:14336\n"
    "ds_read_b128 v[224:227], v242 offset:15360\n"
    "s_waitcnt vmcnt(8)\n"
    "ds_read_b128 v[96:99], v243 offset:0\n"
    "ds_read_b128 v[100:103], v244 offset:0\n"
    "ds_read_b128 v[104:107], v245 offset:0\n"
    "ds_read_b128 v[108:111], v246 offset:0\n"
    "ds_read_b128 v[228:231], v247 offset:0\n"
    "ds_read_b128 v[232:235], v242 offset:0\n"
    "s_waitcnt lgkmcnt(5)\n"
    "v_mfma_f32_32x32x16_bf16 v[144:159], v[72:75], v[96:99], 0\n"
    "ds_read_b128 v[88:91], v242 offset:24576\n"
    "ds_read_b128 v[92:95], v247 offset:32\n"
    "ds_read_b128 v[64:67], v242 offset:1024\n"
    "ds_read_b128 v[68:71], v242 offset:25600\n"
    "s_waitcnt lgkmcnt(5)\n"
    "v_mfma_f32_32x32x16_bf16 v[144:159], v[76:79], v[100:103], v[144:159]\n"
    "s_waitcnt vmcnt(4)\n"
    "ds_read_b128 v[112:115], v243 offset:4096\n"
    "ds_read_b128 v[116:119], v244 offset:4096\n"
    "ds_read_b128 v[120:123], v245 offset:4096\n"
    "v_mfma_f32_32x32x16_bf16 v[144:159], v[80:83], v[104:107], v[144:159]\n"
    "ds_read_b128 v[124:127], v246 offset:4096\n"
    "v_mfma_f32_32x32x16_bf16 v[144:159], v[84:87], v[108:111], v[144:159]\n"
    "s_waitcnt lgkmcnt(8)\n"
    "v_mfma_f32_32x32x16_bf16 v[160:175], v[212:215], v[96:99], 0\n"
    "s_nop 3\n"
    "v_cvt_pk_bf16_f32 v0, v144, v145\n"
    "v_cvt_pk_bf16_f32 v1, v146, v147\n"
    "v_cvt_pk_bf16_f32 v2, v148, v149\n"
    "v_mfma_f32_32x32x16_bf16 v[160:175], v[216:219], v[100:103], v[160:175]\n"
    "s_waitcnt lgkmcnt(4)\n"
    "v_cvt_pk_bf16_f32 v3, v150, v151\n"
    "v_cvt_pk_bf16_f32 v4, v152, v153\n"
    "v_cvt_pk_bf16_f32 v5, v154, v155\n"
    "v_cvt_pk_bf16_f32 v6, v156, v157\n"
    "v_cvt_pk_bf16_f32 v7, v158, v159\n"
    "v_mfma_f32_32x32x16_bf16 v[160:175], v[220:223], v[104:107], v[160:175]\n"
    "s_waitcnt lgkmcnt(2)\n"
    "v_mfma_f32_32x32x16_bf16 v[160:175], v[224:227], v[108:111], v[160:175]\n"
    "s_waitcnt lgkmcnt(0)\n"
    "v_mfma_f32_16x16x32_bf16 v[176:179], v[72:75], v[228:231], 0\n"
    "v_mfma_f32_16x16x32_bf16 v[180:183], v[232:235], v[228:231], 0\n"
    "ds_read_b128 v[232:235], v242 offset:2048\n"
    "v_cvt_pk_bf16_f32 v24, v160, v161\n"
    "v_mfma_f32_16x16x32_bf16 v[204:207], v[88:91], v[228:231], 0\n"
    "ds_read_b128 v[228:231], v247 offset:64\n"
    "v_cvt_pk_bf16_f32 v25, v162, v163\n"
    "v_mfma_f32_16x16x32_bf16 v[176:179], v[76:79], v[92:95], v[176:179]\n"
    "ds_read_b128 v[88:91], v242 offset:26624\n"
    "v_cvt_pk_bf16_f32 v26, v164, v165\n"
    "v_mfma_f32_16x16x32_bf16 v[180:183], v[64:67], v[92:95], v[180:183]\n"
    "ds_read_b128 v[64:67], v242 offset:3072\n"
    "v_cvt_pk_bf16_f32 v27, v166, v167\n"
    "v_mfma_f32_16x16x32_bf16 v[204:207], v[68:71], v[92:95], v[204:207]\n"
    "ds_read_b128 v[92:95], v247 offset:96\n"
    "v_cvt_pk_bf16_f32 v28, v168, v169\n"
    "v_mfma_f32_32x32x16_bf16 v[48:63], v[72:75], v[112:115], 0\n"
    "ds_read_b128 v[68:71], v242 offset:27648\n"
    "v_cvt_pk_bf16_f32 v29, v170, v171\n"
    "v_cvt_pk_bf16_f32 v30, v172, v173\n"
    "v_cvt_pk_bf16_f32 v31, v174, v175\n"
    "v_mfma_f32_32x32x16_bf16 v[48:63], v[76:79], v[116:119], v[48:63]\n"
    "s_waitcnt vmcnt(0)\n"
    "ds_read_b128 v[128:131], v243 offset:8192\n"
    "ds_read_b128 v[132:135], v244 offset:8192\n"
    "ds_read_b128 v[136:139], v245 offset:8192\n"
    "v_mfma_f32_32x32x16_bf16 v[48:63], v[80:83], v[120:123], v[48:63]\n"
    "ds_read_b128 v[140:143], v246 offset:8192\n"
    "v_mfma_f32_32x32x16_bf16 v[48:63], v[84:87], v[124:127], v[48:63]\n"
    "s_waitcnt lgkmcnt(7)\n"
    "v_mfma_f32_16x16x32_bf16 v[176:179], v[80:83], v[228:231], v[176:179]\n"
    "s_waitcnt lgkmcnt(5)\n"
    "v_mfma_f32_16x16x32_bf16 v[180:183], v[232:235], v[228:231], v[180:183]\n"
    "ds_read_b128 v[232:235], v242 offset:4096\n"
    "v_cvt_pk_bf16_f32 v8, v48, v49\n"
    "v_mfma_f32_16x16x32_bf16 v[204:207], v[88:91], v[228:231], v[204:207]\n"
    "ds_read_b128 v[228:231], v248 offset:0\n"
    "v_cvt_pk_bf16_f32 v9, v50, v51\n"
    "v_mfma_f32_16x16x32_bf16 v[176:179], v[84:87], v[92:95], v[176:179]\n"
    "ds_read_b128 v[88:91], v242 offset:28672\n"
    "v_cvt_pk_bf16_f32 v10, v52, v53\n"
    "v_mfma_f32_16x16x32_bf16 v[180:183], v[64:67], v[92:95], v[180:183]\n"
    "ds_read_b128 v[64:67], v242 offset:5120\n"
    "v_cvt_pk_bf16_f32 v11, v54, v55\n"
    "s_waitcnt lgkmcnt(5)\n"
    "v_mfma_f32_16x16x32_bf16 v[204:207], v[68:71], v[92:95], v[204:207]\n"
    "ds_read_b128 v[92:95], v248 offset:32\n"
    "v_cvt_pk_bf16_f32 v12, v56, v57\n"
    "v_mfma_f32_32x32x16_bf16 v[144:159], v[212:215], v[112:115], 0\n"
    "ds_read_b128 v[68:71], v242 offset:29696\n"
    "v_cvt_pk_bf16_f32 v13, v58, v59\n"
    "v_cvt_pk_bf16_f32 v14, v60, v61\n"
    "v_cvt_pk_bf16_f32 v15, v62, v63\n"
    "v_mfma_f32_32x32x16_bf16 v[144:159], v[216:219], v[116:119], v[144:159]\n"
    "s_waitcnt lgkmcnt(6)\n"
    "v_mfma_f32_32x32x16_bf16 v[144:159], v[220:223], v[120:123], v[144:159]\n"
    "s_waitcnt lgkmcnt(5)\n"
    "v_mfma_f32_32x32x16_bf16 v[144:159], v[224:227], v[124:127], v[144:159]\n"
    "s_waitcnt lgkmcnt(3)\n"
    "v_mfma_f32_16x16x32_bf16 v[176:179], v[212:215], v[228:231], v[176:179]\n"
    "s_waitcnt lgkmcnt(1)\n"
    "v_mfma_f32_16x16x32_bf16 v[180:183], v[232:235], v[228:231], v[180:183]\n"
    "ds_read_b128 v[232:235], v242 offset:6144\n"
    "v_cvt_pk_bf16_f32 v32, v144, v145\n"
    "v_mfma_f32_16x16x32_bf16 v[204:207], v[88:91], v[228:231], v[204:207]\n"
    "ds_read_b128 v[228:231], v248 offset:64\n"
    "v_cvt_pk_bf16_f32 v33, v146, v147\n"
    "v_mfma_f32_16x16x32_bf16 v[176:179], v[216:219], v[92:95], v[176:179]\n"
    "ds_read_b128 v[88:91], v242 offset:30720\n"
    "v_cvt_pk_bf16_f32 v34, v148, v149\n"
    "v_mfma_f32_16x16x32_bf16 v[180:183], v[64:67], v[92:95], v[180:183]\n"
    "ds_read_b128 v[64:67], v242 offset:7168\n"
    "v_cvt_pk_bf16_f32 v35, v150, v151\n"
    "s_waitcnt lgkmcnt(4)\n"
    "v_mfma_f32_16x16x32_bf16 v[204:207], v[68:71], v[92:95], v[204:207]\n"
    "ds_read_b128 v[92:95], v248 offset:96\n"
    "v_cvt_pk_bf16_f32 v36, v152, v153\n"
    "v_mfma_f32_32x32x16_bf16 v[160:175], v[72:75], v[128:131], 0\n"
    "ds_read_b128 v[68:71], v242 offset:31744\n"
    "v_cvt_pk_bf16_f32 v37, v154, v155\n"
    "v_cvt_pk_bf16_f32 v38, v156, v157\n"
    "v_cvt_pk_bf16_f32 v39, v158, v159\n"
    "ds_read_b128 v[72:75], v242 offset:16384\n"
    "v_mfma_f32_32x32x16_bf16 v[160:175], v[76:79], v[132:135], v[160:175]\n"
    "ds_read_b128 v[76:79], v242 offset:17408\n"
    "v_mfma_f32_32x32x16_bf16 v[160:175], v[80:83], v[136:139], v[160:175]\n"
    "ds_read_b128 v[80:83], v242 offset:18432\n"
    "v_mfma_f32_32x32x16_bf16 v[160:175], v[84:87], v[140:143], v[160:175]\n"
    "ds_read_b128 v[84:87], v242 offset:19456\n"
    "s_waitcnt lgkmcnt(6)\n"
    "v_mfma_f32_16x16x32_bf16 v[176:179], v[220:223], v[228:231], v[176:179]\n"
    "s_waitcnt lgkmcnt(5)\n"
    "v_mfma_f32_16x16x32_bf16 v[180:183], v[232:235], v[228:231], v[180:183]\n"
    "s_waitcnt lgkmcnt(4)\n"
    "v_cvt_pk_bf16_f32 v16, v160, v161\n"
    "v_cvt_pk_bf16_f32 v17, v162, v163\n"
    "v_mfma_f32_16x16x32_bf16 v[204:207], v[88:91], v[228:231], v[204:207]\n"
    "v_cvt_pk_bf16_f32 v18, v164, v165\n"
    "v_cvt_pk_bf16_f32 v19, v166, v167\n"
    "v_mfma_f32_16x16x32_bf16 v[176:179], v[224:227], v[92:95], v[176:179]\n"
    "s_waitcnt lgkmcnt(2)\n"
    "v_cvt_pk_bf16_f32 v20, v168, v169\n"
    "v_cvt_pk_bf16_f32 v21, v170, v171\n"
    "v_mfma_f32_16x16x32_bf16 v[180:183], v[64:67], v[92:95], v[180:183]\n"
    "v_cvt_pk_bf16_f32 v22, v172, v173\n"
    "v_cvt_pk_bf16_f32 v23, v174, v175\n"
    "v_mfma_f32_16x16x32_bf16 v[204:207], v[68:71], v[92:95], v[204:207]\n"
    "s_waitcnt lgkmcnt(1)\n"
    "v_cvt_pk_bf16_f32 v176, v176, v177\n"
    "v_cvt_pk_bf16_f32 v177, v178, v179\n"
    "v_mfma_f32_32x32x16_bf16 v[48:63], v[212:215], v[128:131], 0\n"
    "ds_write_b64 v250, v[176:177] offset:512\n"
    "v_cvt_pk_bf16_f32 v180, v180, v181\n"
    "v_cvt_pk_bf16_f32 v181, v182, v183\n"
    "v_mfma_f32_32x32x16_bf16 v[48:63], v[216:219], v[132:135], v[48:63]\n"
    "ds_read_b128 v[232:235], v249 offset:512\n"
    "ds_read_b128 v[92:95], v249 offset:544\n"
    "ds_write_b64 v250, v[180:181] offset:1024\n"
    "v_mfma_f32_32x32x16_bf16 v[48:63], v[220:223], v[136:139], v[48:63]\n"
    "ds_read_b128 v[228:231], v247 offset:1024\n"
    "ds_read_b128 v[88:91], v247 offset:1056\n"
    "ds_read_b128 v[212:215], v242 offset:20480\n"
    "ds_read_b128 v[216:219], v242 offset:21504\n"
    "v_mfma_f32_32x32x16_bf16 v[48:63], v[224:227], v[140:143], v[48:63]\n"
    "ds_read_b128 v[220:223], v242 offset:22528\n"
    "ds_read_b128 v[224:227], v242 offset:23552\n"
    "v_mfma_f32_32x32x16_bf16 v[144:159], v[96:99], v[72:75], 0\n"
    "s_waitcnt lgkmcnt(10)\n"
    "s_nop 2\n"
    "v_cvt_pk_bf16_f32 v40, v48, v49\n"
    "v_cvt_pk_bf16_f32 v41, v50, v51\n"
    "v_cvt_pk_bf16_f32 v42, v52, v53\n"
    "v_mfma_f32_32x32x16_bf16 v[144:159], v[100:103], v[76:79], v[144:159]\n"
    "v_cvt_pk_bf16_f32 v43, v54, v55\n"
    "v_cvt_pk_bf16_f32 v44, v56, v57\n"
    "v_cvt_pk_bf16_f32 v45, v58, v59\n"
    "v_cvt_pk_bf16_f32 v46, v60, v61\n"
    "v_cvt_pk_bf16_f32 v47, v62, v63\n"
    "v_mfma_f32_32x32x16_bf16 v[144:159], v[104:107], v[80:83], v[144:159]\n"
    "s_waitcnt lgkmcnt(6)\n"
    "s_waitcnt lgkmcnt(4)\n"
    "v_mfma_f32_16x16x32_bf16 v[176:179], v[0:3], v[228:231], 0\n"
    "s_waitcnt lgkmcnt(3)\n"
    "v_mfma_f32_16x16x32_bf16 v[180:183], v[8:11], v[228:231], 0\n"
    "s_waitcnt lgkmcnt(1)\n"
    "v_mfma_f32_16x16x32_bf16 v[184:187], v[16:19], v[228:231], 0\n"
    "s_waitcnt lgkmcnt(0)\n"
    "v_mfma_f32_16x16x32_bf16 v[188:191], v[232:235], v[228:231], 0\n"
    "ds_read_b128 v[228:231], v248 offset:1088\n"
    "ds_read_b128 v[232:235], v249 offset:576\n"
    "v_mfma_f32_16x16x32_bf16 v[176:179], v[4:7], v[88:91], v[176:179]\n"
    "v_mfma_f32_16x16x32_bf16 v[180:183], v[12:15], v[88:91], v[180:183]\n"
    "v_mfma_f32_16x16x32_bf16 v[184:187], v[20:23], v[88:91], v[184:187]\n"
    "v_mfma_f32_16x16x32_bf16 v[188:191], v[92:95], v[88:91], v[188:191]\n"
    "ds_read_b128 v[88:91], v248 offset:1120\n"
    "ds_read_b128 v[92:95], v249 offset:608\n"
    "v_mfma_f32_32x32x16_bf16 v[144:159], v[108:111], v[84:87], v[144:159]\n"
    "v_mfma_f32_32x32x16_bf16 v[160:175], v[112:115], v[72:75], 0\n"
    "s_nop 3\n"
    "v_cvt_pk_bf16_f32 v48, v144, v145\n"
    "v_cvt_pk_bf16_f32 v49, v146, v147\n"
    "v_cvt_pk_bf16_f32 v50, v148, v149\n"
    "s_waitcnt lgkmcnt(3)\n"
    "v_mfma_f32_16x16x32_bf16 v[176:179], v[24:27], v[228:231], v[176:179]\n"
    "s_waitcnt lgkmcnt(2)\n"
    "v_cvt_pk_bf16_f32 v51, v150, v151\n"
    "v_cvt_pk_bf16_f32 v52, v152, v153\n"
    "v_mfma_f32_16x16x32_bf16 v[180:183], v[32:35], v[228:231], v[180:183]\n"
    "v_cvt_pk_bf16_f32 v53, v154, v155\n"
    "v_cvt_pk_bf16_f32 v54, v156, v157\n"
    "v_mfma_f32_16x16x32_bf16 v[184:187], v[40:43], v[228:231], v[184:187]\n"
    "v_cvt_pk_bf16_f32 v55, v158, v159\n"
    "v_mfma_f32_16x16x32_bf16 v[188:191], v[232:235], v[228:231], v[188:191]\n"
    "ds_read_b128 v[228:231], v249 offset:0\n"
    "ds_read_b128 v[232:235], v249 offset:32\n"
    "s_waitcnt lgkmcnt(2)\n"
    "v_mfma_f32_16x16x32_bf16 v[176:179], v[28:31], v[88:91], v[176:179]\n"
    "v_mfma_f32_16x16x32_bf16 v[180:183], v[36:39], v[88:91], v[180:183]\n"
    "v_mfma_f32_16x16x32_bf16 v[184:187], v[44:47], v[88:91], v[184:187]\n"
    "v_max3_f32 v200, v176, v177, v178\n"
    "v_mfma_f32_16x16x32_bf16 v[188:191], v[92:95], v[88:91], v[188:191]\n"
    "ds_read_b128 v[88:91], v249 offset:64\n"
    "ds_read_b128 v[92:95], v249 offset:96\n"
    "v_mfma_f32_32x32x16_bf16 v[160:175], v[116:119], v[76:79], v[160:175]\n"
    "v_max3_f32 v200, v200, v179, v180\n"
    "v_max3_f32 v200, v200, v181, v182\n"
    "v_max3_f32 v200, v200, v183, v184\n"
    "v_max3_f32 v200, v200, v185, v186\n"
    "v_cndmask_b32_e64 v188, v202, v188, s[54:55]\n"
    "v_cndmask_b32_e64 v189, v202, v189, s[54:55]\n"
    "v_mfma_f32_32x32x16_bf16 v[160:175], v[120:123], v[80:83], v[160:175]\n"
    "v_max3_f32 v200, v200, v187, v188\n"
    "v_max_f32_e32 v200, v200, v189\n"
    "s_nop 1\n"
    "v_max_f32_dpp v200, v200, v200 quad_perm:[1,0,3,2] row_mask:0xf bank_mask:0xf\n"
    "v_mov_b32_e64 v201, v200\n"
    "v_mfma_f32_32x32x16_bf16 v[160:175], v[124:127], v[84:87], v[160:175]\n"
    "s_waitcnt lgkmcnt(2)\n"
    "v_permlane16_swap_b32_e32 v200, v201\n"
    "v_max_f32_e32 v200, v200, v201\n"
    "v_mov_b32_e32 v201, v200\n"
    "s_nop 1\n"
    "v_permlane32_swap_b32_e32 v200, v201\n"
    "v_mfma_f32_32x32x16_bf16 v[144:159], v[128:131], v[72:75], 0\n"
    "v_max_f32_e32 v200, v200, v201\n"
    "v_sub_f32_e32 v176, v176, v200\n"
    "v_sub_f32_e32 v177, v177, v200\n"
    "v_sub_f32_e32 v178, v178, v200\n"
    "v_cvt_pk_bf16_f32 v56, v160, v161\n"
    "v_cvt_pk_bf16_f32 v57, v162, v163\n"
    "v_mfma_f32_32x32x16_bf16 v[144:159], v[132:135], v[76:79], v[144:159]\n"
    "s_waitcnt lgkmcnt(1)\n"
    "v_cvt_pk_bf16_f32 v58, v164, v165\n"
    "v_cvt_pk_bf16_f32 v59, v166, v167\n"
    "v_cvt_pk_bf16_f32 v60, v168, v169\n"
    "v_cvt_pk_bf16_f32 v61, v170, v171\n"
    "v_cvt_pk_bf16_f32 v62, v172, v173\n"
    "v_cvt_pk_bf16_f32 v63, v174, v175\n"
    "v_mfma_f32_32x32x16_bf16 v[144:159], v[136:139], v[80:83], v[144:159]\n"
    "s_waitcnt lgkmcnt(0)\n"
    "v_sub_f32_e32 v179, v179, v200\n"
    "v_sub_f32_e32 v180, v180, v200\n"
    "v_sub_f32_e32 v181, v181, v200\n"
    "v_sub_f32_e32 v182, v182, v200\n"
    "v_sub_f32_e32 v183, v183, v200\n"
    "v_sub_f32_e32 v184, v184, v200\n"
    "v_mfma_f32_32x32x16_bf16 v[144:159], v[140:143], v[84:87], v[144:159]\n"
    "v_sub_f32_e32 v185, v185, v200\n"
    "v_sub_f32_e32 v186, v186, v200\n"
    "v_sub_f32_e32 v187, v187, v200\n"
    "v_sub_f32_e32 v188, v188, v200\n"
    "v_sub_f32_e32 v189, v189, v200\n"
    "v_exp_f32_e32 v176, v176\n"
    "v_mfma_f32_16x16x32_bf16 v[208:211], v[228:231], v[72:75], 0\n"
    "ds_read_b128 v[228:231], v249 offset:0\n"
    "v_exp_f32_e64 v177, v177\n"
    "v_mfma_f32_16x16x32_bf16 v[208:211], v[232:235], v[76:79], v[208:211]\n"
    "ds_read_b128 v[232:235], v249 offset:32\n"
    "v_cvt_pk_bf16_f32 v64, v144, v145\n"
    "v_mfma_f32_16x16x32_bf16 v[208:211], v[88:91], v[80:83], v[208:211]\n"
    "v_cvt_pk_bf16_f32 v65, v146, v147\n"
    "v_cvt_pk_bf16_f32 v66, v148, v149\n"
    "v_mfma_f32_16x16x32_bf16 v[208:211], v[92:95], v[84:87], v[208:211]\n"
    "v_cvt_pk_bf16_f32 v67, v150, v151\n"
    "v_cvt_pk_bf16_f32 v68, v152, v153\n"
    "v_mfma_f32_32x32x16_bf16 v[160:175], v[96:99], v[212:215], 0\n"
    "v_cvt_pk_bf16_f32 v69, v154, v155\n"
    "v_cvt_pk_bf16_f32 v70, v156, v157\n"
    "v_cvt_pk_bf16_f32 v71, v158, v159\n"
    "v_exp_f32_e64 v178, v178\n"
    "v_cvt_pk_bf16_f32 v192, v208, v209\n"
    "v_mfma_f32_32x32x16_bf16 v[160:175], v[100:103], v[216:219], v[160:175]\n"
    "v_exp_f32_e32 v179, v179\n"
    "v_exp_f32_e32 v180, v180\n"
    "v_exp_f32_e64 v181, v181\n"
    "v_mfma_f32_32x32x16_bf16 v[160:175], v[104:107], v[220:223], v[160:175]\n"
    "v_exp_f32_e32 v182, v182\n"
    "v_exp_f32_e32 v183, v183\n"
    "v_exp_f32_e64 v184, v184\n"
    "v_mfma_f32_32x32x16_bf16 v[160:175], v[108:111], v[224:227], v[160:175]\n"
    "s_waitcnt lgkmcnt(0)\n"
    "v_exp_f32_e32 v185, v185\n"
    "v_exp_f32_e32 v186, v186\n"
    "v_exp_f32_e32 v187, v187\n"
    "v_mfma_f32_16x16x32_bf16 v[208:211], v[228:231], v[212:215], 0\n"
    "ds_read_b128 v[228:231], v249 offset:64\n"
    "v_exp_f32_e64 v188, v188\n"
    "v_mfma_f32_16x16x32_bf16 v[208:211], v[232:235], v[216:219], v[208:211]\n"
    "ds_read_b128 v[232:235], v249 offset:96\n"
    "v_cvt_pk_bf16_f32 v72, v160, v161\n"
    "v_mfma_f32_32x32x16_bf16 v[144:159], v[112:115], v[212:215], 0\n"
    "v_cvt_pk_bf16_f32 v73, v162, v163\n"
    "v_cvt_pk_bf16_f32 v74, v164, v165\n"
    "v_cvt_pk_bf16_f32 v75, v166, v167\n"
    "v_cvt_pk_bf16_f32 v76, v168, v169\n"
    "v_cvt_pk_bf16_f32 v77, v170, v171\n"
    "v_cvt_pk_bf16_f32 v78, v172, v173\n"
    "v_mfma_f32_32x32x16_bf16 v[144:159], v[116:119], v[216:219], v[144:159]\n"
    "v_cvt_pk_bf16_f32 v79, v174, v175\n"
    "v_exp_f32_e32 v189, v189\n"
    "s_nop 0\n"
    "v_add_f32_e32 v203, v188, v189\n"
    "v_mul_f32_e32 v203, 0.5, v203\n"
    "v_mfma_f32_32x32x16_bf16 v[144:159], v[120:123], v[220:223], v[144:159]\n"
    "v_add_f32_e32 v203, v176, v203\n"
    "v_add_f32_e32 v203, v177, v203\n"
    "v_add_f32_e32 v203, v178, v203\n"
    "v_add_f32_e32 v203, v179, v203\n"
    "v_add_f32_e32 v203, v180, v203\n"
    "v_add_f32_e32 v203, v181, v203\n"
    "v_mfma_f32_32x32x16_bf16 v[144:159], v[124:127], v[224:227], v[144:159]\n"
    "v_add_f32_e32 v203, v182, v203\n"
    "v_add_f32_e32 v203, v183, v203\n"
    "v_add_f32_e32 v203, v184, v203\n"
    "v_add_f32_e32 v203, v185, v203\n"
    "v_add_f32_e32 v203, v186, v203\n"
    "v_add_f32_e64 v203, v187, v203\n"
    "s_waitcnt lgkmcnt(1)\n"
    "v_mfma_f32_16x16x32_bf16 v[208:211], v[228:231], v[220:223], v[208:211]\n"
    "ds_read_b128 v[228:231], v242 offset:0\n"
    "v_cvt_pk_bf16_f32 v176, v176, v177\n"
    "s_waitcnt lgkmcnt(1)\n"
    "v_mfma_f32_16x16x32_bf16 v[208:211], v[232:235], v[224:227], v[208:211]\n"
    "s_cmp_lt_i32 s37, s36\n"
    "s_cbranch_scc0 .Lskip0_%=\n"
    "s_add_u32 s50, s40, 12288\n"
    "s_addc_u32 s51, s41, 0\n"
    "s_mov_b32 m0, s53\n"
    "s_mov_b64 exec, s[62:63]\n"
    "global_load_lds_dwordx4 v242, s[50:51]\n"
    "s_mov_b64 exec, -1\n"
    ".Lskip0_%=:\n"
    "v_mfma_f32_32x32x16_bf16 v[160:175], v[128:131], v[212:215], 0\n"
    "ds_read_b128 v[232:235], v242 offset:1024\n"
    "v_cvt_pk_bf16_f32 v177, v178, v179\n"
    "v_cvt_pk_bf16_f32 v180, v180, v181\n"
    "v_cvt_pk_bf16_f32 v181, v182, v183\n"
    "v_cvt_pk_bf16_f32 v184, v184, v185\n"
    "v_cvt_pk_bf16_f32 v185, v186, v187\n"
    "v_mfma_f32_32x32x16_bf16 v[160:175], v[132:135], v[216:219], v[160:175]\n"
    "ds_write_b64 v250, v[176:177] offset:1536\n"
    "v_cvt_pk_bf16_f32 v188, v188, v189\n"
    "v_add_f32_dpp v203, v203, v203 quad_perm:[1,0,3,2] row_mask:0xf bank_mask:0xf\n"
    "v_mfma_f32_32x32x16_bf16 v[160:175], v[136:139], v[220:223], v[160:175]\n"
    "ds_write_b64 v250, v[180:181] offset:2048\n"
    "v_mov_b32_e32 v201, v203\n"
    "v_cvt_pk_bf16_f32 v194, v208, v209\n"
    "v_mfma_f32_32x32x16_bf16 v[160:175], v[140:143], v[224:227], v[160:175]\n"
    "ds_write_b64 v250, v[184:185] offset:2560\n"
    "v_permlane16_swap_b32_e32 v203, v201\n"
    "v_add_f32_e32 v203, v203, v201\n"
    "v_mov_b32_e64 v201, v203\n"
    "v_cvt_pk_bf16_f32 v80, v144, v145\n"
    "v_cvt_pk_bf16_f32 v81, v146, v147\n"
    "v_permlane32_swap_b32_e32 v203, v201\n"
    "v_add_f32_e32 v203, v203, v201\n"
    "v_rcp_f32_e64 v203, v203\n"
    "v_cvt_pk_bf16_f32 v82, v148, v149\n"
    "v_cvt_pk_bf16_f32 v83, v150, v151\n"
    "v_cvt_pk_bf16_f32 v84, v152, v153\n"
    "v_cvt_pk_bf16_f32 v85, v154, v155\n"
    "v_cvt_pk_bf16_f32 v86, v156, v157\n"
    "v_cvt_pk_bf16_f32 v87, v158, v159\n"
    "s_waitcnt lgkmcnt(2)\n"
    "v_mfma_f32_32x32x16_bf16 v[144:159], v[228:231], v[96:99], 0\n"
    "ds_read_b128 v[212:215], v242 offset:2048\n"
    "v_cvt_pk_bf16_f32 v88, v160, v161\n"
    "v_cvt_pk_bf16_f32 v89, v162, v163\n"
    "v_cvt_pk_bf16_f32 v90, v164, v165\n"
    "v_cvt_pk_bf16_f32 v91, v166, v167\n"
    "v_cvt_pk_bf16_f32 v92, v168, v169\n"
    "v_mfma_f32_32x32x16_bf16 v[144:159], v[232:235], v[100:103], v[144:159]\n"
    "ds_read_b128 v[216:219], v242 offset:3072\n"
    "v_cvt_pk_bf16_f32 v93, v170, v171\n"
    "v_cvt_pk_bf16_f32 v94, v172, v173\n"
    "v_cvt_pk_bf16_f32 v95, v174, v175\n"
    "v_cndmask_b32_e64 v196, 0, v188, s[56:57]\n"
    "ds_read_b128 v[220:223], v242 offset:4096\n"
    "ds_read_b128 v[224:227], v242 offset:5120\n"
    "v_mfma_f32_16x16x16_bf16 v[208:211], v[192:193], v[196:197], 0\n"
    "v_cndmask_b32_e64 v196, v188, 0, s[56:57]\n"
    "ds_read_b128 v[228:231], v242 offset:6144\n"
    "ds_read_b128 v[232:235], v242 offset:7168\n"
    "v_mfma_f32_16x16x16_bf16 v[208:211], v[194:195], v[196:197], v[208:211]\n"
    "s_waitcnt lgkmcnt(5)\n"
    "v_mfma_f32_32x32x16_bf16 v[144:159], v[212:215], v[104:107], v[144:159]\n"
    "ds_read_b128 v[212:215], v249 offset:512\n"
    "s_waitcnt lgkmcnt(5)\n"
    "v_mfma_f32_32x32x16_bf16 v[144:159], v[216:219], v[108:111], v[144:159]\n"
    "ds_read_b128 v[216:219], v249 offset:544\n"
    "s_waitcnt lgkmcnt(5)\n"
    "v_mfma_f32_32x32x16_bf16 v[160:175], v[220:223], v[96:99], 0\n"
    "ds_read_b128 v[220:223], v247 offset:1536\n"
    "s_nop 2\n"
    "v_cvt_pk_bf16_f32 v176, v144, v145\n"
    "v_cvt_pk_bf16_f32 v177, v146, v147\n"
    "s_waitcnt lgkmcnt(3)\n"
    "v_mfma_f32_32x32x16_bf16 v[160:175], v[224:227], v[100:103], v[160:175]\n"
    "ds_read_b128 v[224:227], v247 offset:1568\n"
    "v_cvt_pk_bf16_f32 v178, v148, v149\n"
    "v_cvt_pk_bf16_f32 v179, v150, v151\n"
    "v_cvt_pk_bf16_f32 v180, v152, v153\n"
    "v_cvt_pk_bf16_f32 v181, v154, v155\n"
    "v_cvt_pk_bf16_f32 v182, v156, v157\n"
    "v_mfma_f32_32x32x16_bf16 v[160:175], v[228:231], v[104:107], v[160:175]\n"
    "ds_read_b128 v[228:231], v247 offset:2048\n"
    "v_cvt_pk_bf16_f32 v183, v158, v159\n"
    "v_mfma_f32_32x32x16_bf16 v[160:175], v[232:235], v[108:111], v[160:175]\n"
    "ds_read_b128 v[232:235], v247 offset:2080\n"
    "v_mfma_f32_32x32x16_bf16 v[112:127], v[8:11], v[176:179], 0\n"
    "s_waitcnt lgkmcnt(4)\n"
    "v_mfma_f32_32x32x16_bf16 v[112:127], v[12:15], v[180:183], v[112:127]\n"
    "s_waitcnt lgkmcnt(3)\n"
    "v_mfma_f32_32x32x16_bf16 v[128:143], v[16:19], v[176:179], 0\n"
    "s_waitcnt lgkmcnt(2)\n"
    "s_nop 2\n"
    "v_max3_f32 v200, v112, v113, v114\n"
    "v_max3_f32 v200, v200, v115, v115\n"
    "v_max3_f32 v200, v200, v116, v117\n"
    "v_mfma_f32_32x32x16_bf16 v[128:143], v[20:23], v[180:183], v[128:143]\n"
    "s_waitcnt lgkmcnt(1)\n"
    "v_max3_f32 v200, v200, v118, v119\n"
    "v_max3_f32 v200, v200, v120, v121\n"
    "v_max3_f32 v200, v200, v122, v123\n"
    "v_max3_f32 v200, v200, v124, v125\n"
    "v_max3_f32 v200, v200, v126, v127\n"
    "v_mfma_f32_32x32x16_bf16 v[96:111], v[0:3], v[176:179], 0\n"
    "s_waitcnt lgkmcnt(0)\n"
    "s_nop 2\n"
    "v_max3_f32 v200, v200, v128, v129\n"
    "v_max3_f32 v200, v200, v130, v131\n"
    "v_max3_f32 v200, v200, v132, v133\n"
    "v_mfma_f32_32x32x16_bf16 v[96:111], v[4:7], v[180:183], v[96:111]\n"
    "v_max3_f32 v200, v200, v134, v135\n"
    "v_max3_f32 v200, v200, v136, v137\n"
    "v_max3_f32 v200, v200, v138, v139\n"
    "v_max3_f32 v200, v200, v140, v141\n"
    "v_max3_f32 v200, v200, v142, v143\n"
    "v_mfma_f32_16x16x32_bf16 v[184:187], v[212:215], v[176:179], 0\n"
    "ds_read_b128 v[212:215], v247 offset:2560\n"
    "v_mfma_f32_16x16x32_bf16 v[184:187], v[216:219], v[180:183], v[184:187]\n"
    "ds_read_b128 v[216:219], v247 offset:2592\n"
    "v_mfma_f32_16x16x32_bf16 v[208:211], v[48:51], v[220:223], v[208:211]\n"
    "ds_read_b128 v[220:223], v248 offset:1600\n"
    "v_mfma_f32_16x16x32_bf16 v[208:211], v[52:55], v[224:227], v[208:211]\n"
    "ds_read_b128 v[224:227], v248 offset:1632\n"
    "v_max3_f32 v200, v200, v184, v185\n"
    "v_mfma_f32_16x16x32_bf16 v[208:211], v[56:59], v[228:231], v[208:211]\n"
    "ds_read_b128 v[228:231], v248 offset:2112\n"
    "v_max3_f32 v200, v200, v96, v97\n"
    "v_mfma_f32_16x16x32_bf16 v[208:211], v[60:63], v[232:235], v[208:211]\n"
    "ds_read_b128 v[232:235], v248 offset:2144\n"
    "v_max3_f32 v200, v200, v98, v99\n"
    "v_max3_f32 v200, v200, v100, v101\n"
    "v_max3_f32 v200, v200, v102, v103\n"
    "v_max3_f32 v200, v200, v104, v105\n"
    "v_max3_f32 v200, v200, v106, v107\n"
    "v_max3_f32 v200, v200, v108, v109\n"
    "v_max3_f32 v200, v200, v110, v111\n"
    "v_mov_b32_e32 v201, v200\n"
    "s_nop 1\n"
    "v_permlane32_swap_b32_e32 v200, v201\n"
    "v_max_f32_e32 v200, v200, v201\n"
    "v_and_b32_e32 v201, s59, v200\n"
    "v_sub_f32_e32 v201, v200, v201\n"
    "s_waitcnt lgkmcnt(5)\n"
    "v_mfma_f32_16x16x32_bf16 v[208:211], v[64:67], v[212:215], v[208:211]\n"
    "ds_read_b128 v[212:215], v248 offset:2624\n"
    "v_perm_b32 v198, v201, v200, s58\n"
    "s_waitcnt lgkmcnt(5)\n"
    "v_mfma_f32_16x16x32_bf16 v[208:211], v[68:71], v[216:219], v[208:211]\n"
    "ds_read_b128 v[216:219], v248 offset:2656\n"
    "v_and_b32_e64 v199, s59, v201\n"
    "s_waitcnt lgkmcnt(5)\n"
    "v_mfma_f32_16x16x32_bf16 v[208:211], v[72:75], v[220:223], v[208:211]\n"
    "ds_read_b128 v[220:223], v242 offset:0\n"
    "v_sub_f32_e32 v199, v201, v199\n"
    "s_waitcnt lgkmcnt(5)\n"
    "v_mfma_f32_16x16x32_bf16 v[208:211], v[76:79], v[224:227], v[208:211]\n"
    "ds_read_b128 v[224:227], v243 offset:4096\n"
    "v_lshrrev_b32_e32 v199, 16, v199\n"
    "s_waitcnt lgkmcnt(4)\n"
    "v_mfma_f32_16x16x32_bf16 v[208:211], v[80:83], v[228:231], v[208:211]\n"
    "ds_read_b128 v[228:231], v242 offset:1024\n"
    "v_sub_f32_e64 v184, v184, v200\n"
    "v_mfma_f32_16x16x32_bf16 v[208:211], v[84:87], v[232:235], v[208:211]\n"
    "ds_read_b128 v[232:235], v244 offset:4096\n"
    "v_sub_f32_e64 v185, v185, v200\n"
    "v_mfma_f32_32x32x8_bf16 v[96:111], v[240:241], v[198:199], v[96:111]\n"
    "v_exp_f32_e32 v184, v184\n"
    "v_exp_f32_e64 v185, v185\n"
    "s_nop 0\n"
    "v_cvt_pk_bf16_f32 v196, v184, v185\n"
    "v_mfma_f32_32x32x8_bf16 v[112:127], v[240:241], v[198:199], v[112:127]\n"
    "s_nop 3\n"
    "v_exp_f32_e32 v96, v96\n"
    "v_exp_f32_e32 v97, v97\n"
    "s_waitcnt lgkmcnt(4)\n"
    "v_mfma_f32_16x16x32_bf16 v[208:211], v[88:91], v[212:215], v[208:211]\n"
    "ds_read_b128 v[212:215], v242 offset:2048\n"
    "v_exp_f32_e64 v98, v98\n"
    "v_mfma_f32_16x16x32_bf16 v[208:211], v[92:95], v[216:219], v[208:211]\n"
    "ds_read_b128 v[216:219], v245 offset:4096\n"
    "v_exp_f32_e64 v99, v99\n"
    "v_mfma_f32_32x32x8_bf16 v[128:143], v[240:241], v[198:199], v[128:143]\n"
    "v_exp_f32_e32 v100, v100\n"
    "v_exp_f32_e32 v101, v101\n"
    "v_exp_f32_e32 v102, v102\n"
    "v_exp_f32_e32 v103, v103\n"
    "v_mfma_f32_16x16x16_bf16 v[188:191], v[236:237], v[196:197], 0\n"
    "v_fmac_f32_e32 v204, v208, v203\n"
    "v_fmac_f32_e32 v205, v209, v203\n"
    "v_fmac_f32_e32 v206, v210, v203\n"
    "s_waitcnt lgkmcnt(2)\n"
    "v_mfma_f32_32x32x16_bf16 v[144:159], v[220:223], v[224:227], 0\n"
    "ds_read_b128 v[220:223], v242 offset:3072\n"
    "v_fmac_f32_e64 v207, v211, v203\n"
    "v_cvt_pk_bf16_f32 v204, v204, v205\n"
    "v_cvt_pk_bf16_f32 v205, v206, v207\n"
    "v_pk_max_i16 v204, v204, 0\n"
    "v_pk_max_i16 v205, v205, 0\n"
    "v_mfma_f32_32x32x16_bf16 v[144:159], v[228:231], v[232:235], v[144:159]\n"
    "global_store_dwordx2 v250, v[204:205], s[48:49]\n"
    "v_cvt_pk_bf16_f32 v206, v164, v165\n"
    "v_cvt_pk_bf16_f32 v207, v166, v167\n"
    "v_cvt_pk_bf16_f32 v204, v160, v161\n"
    "v_cvt_pk_bf16_f32 v205, v162, v163\n"
    "v_cvt_pk_bf16_f32 v208, v168, v169\n"
    "v_cvt_pk_bf16_f32 v209, v170, v171\n"
    "v_cvt_pk_bf16_f32 v210, v172, v173\n"
    "s_waitcnt lgkmcnt(1)\n"
    "v_mfma_f32_32x32x16_bf16 v[144:159], v[212:215], v[216:219], v[144:159]\n"
    "ds_read_b128 v[224:227], v246 offset:4096\n"
    "v_cvt_pk_bf16_f32 v211, v174, v175\n"
    "v_cvt_pk_bf16_f32 v96, v96, v97\n"
    "v_cvt_pk_bf16_f32 v97, v98, v99\n"
    "v_cvt_pk_bf16_f32 v98, v100, v101\n"
    "v_cvt_pk_bf16_f32 v99, v102, v103\n"
    "v_mfma_f32_32x32x8_bf16 v[160:175], v[192:193], v[196:197], 0\n"
    "ds_read_b128 v[228:231], v249 offset:576\n"
    "ds_read_b128 v[232:235], v249 offset:608\n"
    "v_exp_f32_e32 v104, v104\n"
    "v_exp_f32_e64 v105, v105\n"
    "v_mfma_f32_16x16x32_bf16 v[188:191], v[236:239], v[96:99], v[188:191]\n"
    "ds_read_b128 v[212:215], v242 offset:24576\n"
    "v_exp_f32_e64 v106, v106\n"
    "v_mfma_f32_32x32x16_bf16 v[160:175], v[48:51], v[96:99], v[160:175]\n"
    "ds_read_b128 v[216:219], v243 offset:0\n"
    "v_exp_f32_e32 v107, v107\n"
    "v_exp_f32_e32 v108, v108\n"
    "v_exp_f32_e32 v109, v109\n"
    "v_exp_f32_e32 v110, v110\n"
    "v_exp_f32_e64 v111, v111\n"
    "v_cvt_pk_bf16_f32 v104, v104, v105\n"
    "v_cvt_pk_bf16_f32 v105, v106, v107\n"
    "s_waitcnt lgkmcnt(4)\n"
    "v_mfma_f32_32x32x16_bf16 v[144:159], v[220:223], v[224:227], v[144:159]\n"
    "ds_read_b128 v[220:223], v242 offset:25600\n"
    "v_cvt_pk_bf16_f32 v106, v108, v109\n"
    "v_cvt_pk_bf16_f32 v107, v110, v111\n"
    "v_exp_f32_e32 v112, v112\n"
    "v_exp_f32_e32 v113, v113\n"
    "s_waitcnt lgkmcnt(3)\n"
    "v_mfma_f32_16x16x32_bf16 v[184:187], v[228:231], v[204:207], 0\n"
    "ds_read_b128 v[224:227], v244 offset:0\n"
    "v_exp_f32_e64 v114, v114\n"
    "v_mfma_f32_16x16x32_bf16 v[184:187], v[232:235], v[208:211], v[184:187]\n"
    "ds_read_b128 v[228:231], v242 offset:26624\n"
    "v_cvt_pk_bf16_f32 v176, v144, v145\n"
    "v_mfma_f32_32x32x16_bf16 v[160:175], v[52:55], v[104:107], v[160:175]\n"
    "ds_read_b128 v[232:235], v245 offset:0\n"
    "v_cvt_pk_bf16_f32 v177, v146, v147\n"
    "v_cvt_pk_bf16_f32 v178, v148, v149\n"
    "v_cvt_pk_bf16_f32 v179, v150, v151\n"
    "v_cvt_pk_bf16_f32 v180, v152, v153\n"
    "v_cvt_pk_bf16_f32 v181, v154, v155\n"
    "v_mfma_f32_16x16x32_bf16 v[188:191], v[236:239], v[104:107], v[188:191]\n"
    "s_waitcnt lgkmcnt(4)\n"
    "v_cvt_pk_bf16_f32 v182, v156, v157\n"
    "v_cvt_pk_bf16_f32 v183, v158, v159\n"
    "v_mfma_f32_32x32x16_bf16 v[96:111], v[24:27], v[204:207], 0\n"
    "v_exp_f32_e32 v115, v115\n"
    "v_exp_f32_e32 v116, v116\n"
    "v_exp_f32_e32 v117, v117\n"
    "v_mfma_f32_32x32x16_bf16 v[96:111], v[28:31], v[208:211], v[96:111]\n"
    "s_waitcnt lgkmcnt(3)\n"
    "v_exp_f32_e32 v118, v118\n"
    "v_exp_f32_e64 v119, v119\n"
    "v_cvt_pk_bf16_f32 v112, v112, v113\n"
    "v_cvt_pk_bf16_f32 v113, v114, v115\n"
    "v_mfma_f32_32x32x16_bf16 v[144:159], v[212:215], v[216:219], 0\n"
    "ds_read_b128 v[212:215], v242 offset:27648\n"
    "v_cvt_pk_bf16_f32 v114, v116, v117\n"
    "v_cvt_pk_bf16_f32 v115, v118, v119\n"
    "v_exp_f32_e32 v120, v120\n"
    "v_exp_f32_e32 v121, v121\n"
    "v_mfma_f32_32x32x16_bf16 v[160:175], v[56:59], v[112:115], v[160:175]\n"
    "ds_read_b128 v[216:219], v246 offset:0\n"
    "v_exp_f32_e32 v122, v122\n"
    "v_exp_f32_e32 v123, v123\n"
    "v_exp_f32_e64 v124, v124\n"
    "v_mfma_f32_16x16x32_bf16 v[188:191], v[236:239], v[112:115], v[188:191]\n"
    "s_waitcnt lgkmcnt(2)\n"
    "v_exp_f32_e32 v125, v125\n"
    "v_mfma_f32_32x32x16_bf16 v[144:159], v[220:223], v[224:227], v[144:159]\n"
    "ds_read_b128 v[224:227], v242 offset:4096\n"
    "v_exp_f32_e32 v126, v126\n"
    "v_exp_f32_e32 v127, v127\n"
    "v_cvt_pk_bf16_f32 v120, v120, v121\n"
    "v_mfma_f32_32x32x16_bf16 v[144:159], v[228:231], v[232:235], v[144:159]\n"
    "ds_read_b128 v[228:231], v243 offset:4096\n"
    "v_cvt_pk_bf16_f32 v121, v122, v123\n"
    "v_cvt_pk_bf16_f32 v122, v124, v125\n"
    "v_cvt_pk_bf16_f32 v123, v126, v127\n"
    "v_exp_f32_e32 v128, v128\n"
    "v_exp_f32_e32 v129, v129\n"
    "v_mfma_f32_32x32x16_bf16 v[160:175], v[60:63], v[120:123], v[160:175]\n"
    "ds_read_b128 v[232:235], v242 offset:5120\n"
    "v_exp_f32_e32 v130, v130\n"
    "v_exp_f32_e32 v131, v131\n"
    "v_exp_f32_e64 v132, v132\n"
    "v_mfma_f32_16x16x32_bf16 v[188:191], v[236:239], v[120:123], v[188:191]\n"
    "s_waitcnt lgkmcnt(3)\n"
    "v_exp_f32_e32 v133, v133\n"
    "v_mfma_f32_32x32x16_bf16 v[112:127], v[32:35], v[204:207], 0\n"
    "v_exp_f32_e32 v134, v134\n"
    "v_exp_f32_e32 v135, v135\n"
    "v_cvt_pk_bf16_f32 v128, v128, v129\n"
    "v_cvt_pk_bf16_f32 v129, v130, v131\n"
    "v_mfma_f32_32x32x16_bf16 v[112:127], v[36:39], v[208:211], v[112:127]\n"
    "s_waitcnt lgkmcnt(2)\n"
    "v_cvt_pk_bf16_f32 v130, v132, v133\n"
    "v_cvt_pk_bf16_f32 v131, v134, v135\n"
    "v_exp_f32_e32 v136, v136\n"
    "v_exp_f32_e64 v137, v137\n"
    "v_mfma_f32_32x32x16_bf16 v[144:159], v[212:215], v[216:219], v[144:159]\n"
    "ds_read_b128 v[212:215], v244 offset:4096\n"
    "v_exp_f32_e32 v138, v138\n"
    "v_exp_f32_e32 v139, v139\n"
    "v_exp_f32_e64 v140, v140\n"
    "v_mfma_f32_32x32x16_bf16 v[160:175], v[64:67], v[128:131], v[160:175]\n"
    "ds_read_b128 v[216:219], v242 offset:6144\n"
    "v_exp_f32_e32 v141, v141\n"
    "v_exp_f32_e32 v142, v142\n"
    "v_exp_f32_e64 v143, v143\n"
    "v_mfma_f32_16x16x32_bf16 v[188:191], v[236:239], v[128:131], v[188:191]\n"
    "s_waitcnt lgkmcnt(2)\n"
    "v_cvt_pk_bf16_f32 v220, v136, v137\n"
    "v_cvt_pk_bf16_f32 v221, v138, v139\n"
    "v_cvt_pk_bf16_f32 v222, v140, v141\n"
    "v_cvt_pk_bf16_f32 v223, v142, v143\n"
    "v_mfma_f32_32x32x16_bf16 v[128:143], v[40:43], v[204:207], 0\n"
    "v_max3_f32 v200, v96, v97, v98\n"
    "v_max3_f32 v200, v200, v99, v99\n"
    "v_max3_f32 v200, v200, v100, v101\n"
    "v_max3_f32 v200, v200, v102, v103\n"
    "v_max3_f32 v200, v200, v104, v105\n"
    "v_max3_f32 v200, v200, v106, v107\n"
    "v_mfma_f32_32x32x16_bf16 v[128:143], v[44:47], v[208:211], v[128:143]\n"
    "v_max3_f32 v200, v200, v108, v109\n"
    "v_max3_f32 v200, v200, v110, v111\n"
    "v_max3_f32 v200, v200, v112, v113\n"
    "v_max3_f32 v200, v200, v114, v115\n"
    "v_max3_f32 v200, v200, v116, v117\n"
    "v_max3_f32 v200, v200, v118, v119\n"
    "v_mfma_f32_32x32x16_bf16 v[160:175], v[68:71], v[220:223], v[160:175]\n"
    "s_waitcnt lgkmcnt(1)\n"
    "v_max3_f32 v200, v200, v120, v121\n"
    "v_max3_f32 v200, v200, v122, v123\n"
    "v_max3_f32 v200, v200, v124, v125\n"
    "v_max3_f32 v200, v200, v126, v127\n"
    "v_max3_f32 v200, v200, v184, v185\n"
    "v_max3_f32 v200, v200, v128, v129\n"
    "v_mfma_f32_16x16x32_bf16 v[188:191], v[236:239], v[220:223], v[188:191]\n"
    "ds_read_b128 v[220:223], v245 offset:4096\n"
    "v_max3_f32 v200, v200, v130, v131\n"
    "v_max3_f32 v200, v200, v132, v133\n"
    "v_max3_f32 v200, v200, v134, v135\n"
    "v_max3_f32 v200, v200, v136, v137\n"
    "v_max3_f32 v200, v200, v138, v139\n"
    "v_max3_f32 v200, v200, v140, v141\n"
    "v_max3_f32 v200, v200, v142, v143\n"
    "v_mov_b32_e32 v201, v200\n"
    "v_rcp_f32_e32 v203, v188\n"
    "s_nop 0\n"
    "v_permlane32_swap_b32_e32 v200, v201\n"
    "v_max_f32_e32 v200, v200, v201\n"
    "v_and_b32_e32 v201, s59, v200\n"
    "v_sub_f32_e64 v201, v200, v201\n"
    "v_perm_b32 v198, v201, v200, s58\n"
    "v_and_b32_e32 v199, s59, v201\n"
    "v_sub_f32_e32 v199, v201, v199\n"
    "v_lshrrev_b32_e32 v199, 16, v199\n"
    "v_sub_f32_e32 v184, v184, v200\n"
    "v_sub_f32_e64 v185, v185, v200\n"
    "v_mfma_f32_32x32x8_bf16 v[96:111], v[240:241], v[198:199], v[96:111]\n"
    "v_exp_f32_e32 v184, v184\n"
    "v_exp_f32_e32 v185, v185\n"
    "v_fmac_f32_e32 v144, v160, v203\n"
    "v_fmac_f32_e32 v145, v161, v203\n"
    "v_fmac_f32_e64 v146, v162, v203\n"
    "v_mfma_f32_32x32x8_bf16 v[112:127], v[240:241], v[198:199], v[112:127]\n"
    "v_fmac_f32_e32 v147, v163, v203\n"
    "v_fmac_f32_e32 v148, v164, v203\n"
    "v_fmac_f32_e32 v149, v165, v203\n"
    "v_fmac_f32_e32 v150, v166, v203\n"
    "v_fmac_f32_e32 v151, v167, v203\n"
    "v_fmac_f32_e32 v152, v168, v203\n"
    "v_fmac_f32_e64 v153, v169, v203\n"
    "v_mfma_f32_32x32x8_bf16 v[128:143], v[240:241], v[198:199], v[128:143]\n"
    "v_fmac_f32_e32 v154, v170, v203\n"
    "v_fmac_f32_e32 v155, v171, v203\n"
    "v_fmac_f32_e32 v156, v172, v203\n"
    "v_fmac_f32_e32 v157, v173, v203\n"
    "v_fmac_f32_e32 v158, v174, v203\n"
    "v_fmac_f32_e32 v159, v175, v203\n"
    "v_cvt_pk_bf16_f32 v144, v144, v145\n"
    "v_mfma_f32_32x32x16_bf16 v[160:175], v[224:227], v[228:231], 0\n"
    "ds_read_b128 v[224:227], v242 offset:7168\n"
    "v_cvt_pk_bf16_f32 v145, v146, v147\n"
    "v_cvt_pk_bf16_f32 v146, v148, v149\n"
    "v_cvt_pk_bf16_f32 v147, v150, v151\n"
    "v_pk_max_i16 v144, v144, 0\n"
    "v_pk_max_i16 v145, v145, 0\n"
    "v_mfma_f32_32x32x16_bf16 v[160:175], v[232:235], v[212:215], v[160:175]\n"
    "ds_read_b128 v[228:231], v246 offset:4096\n"
    "v_pk_max_i16 v146, v146, 0\n"
    "v_pk_max_i16 v147, v147, 0\n"
    "v_cvt_pk_bf16_f32 v152, v152, v153\n"
    "v_cvt_pk_bf16_f32 v153, v154, v155\n"
    "v_cvt_pk_bf16_f32 v154, v156, v157\n"
    "s_waitcnt lgkmcnt(2)\n"
    "v_mfma_f32_32x32x16_bf16 v[160:175], v[216:219], v[220:223], v[160:175]\n"
    "global_store_dwordx4 v251, v[144:147], s[44:45] offset:-4096\n"
    "v_cvt_pk_bf16_f32 v155, v158, v159\n"
    "v_pk_max_i16 v152, v152, 0\n"
    "v_pk_max_i16 v153, v153, 0\n"
    "v_pk_max_i16 v154, v154, 0\n"
    "v_pk_max_i16 v155, v155, 0\n"
    "v_cvt_pk_bf16_f32 v196, v184, v185\n"
    "v_exp_f32_e32 v96, v96\n"
    "v_exp_f32_e64 v97, v97\n"
    "v_mfma_f32_16x16x16_bf16 v[188:191], v[236:237], v[196:197], 0\n"
    "v_exp_f32_e32 v98, v98\n"
    "v_exp_f32_e32 v99, v99\n"
    "v_exp_f32_e32 v100, v100\n"
    "v_exp_f32_e32 v101, v101\n"
    "v_exp_f32_e32 v102, v102\n"
    "v_exp_f32_e32 v103, v103\n"
    "v_cvt_pk_bf16_f32 v96, v96, v97\n"
    "v_cvt_pk_bf16_f32 v97, v98, v99\n"
    "s_waitcnt lgkmcnt(0)\n"
    "v_mfma_f32_32x32x16_bf16 v[160:175], v[224:227], v[228:231], v[160:175]\n"
    "ds_read_b128 v[232:235], v249 offset:512\n"
    "v_cvt_pk_bf16_f32 v98, v100, v101\n"
    "v_cvt_pk_bf16_f32 v99, v102, v103\n"
    "ds_read_b128 v[212:215], v249 offset:544\n"
    "v_exp_f32_e32 v104, v104\n"
    "v_mfma_f32_16x16x32_bf16 v[188:191], v[236:239], v[96:99], v[188:191]\n"
    "global_store_dwordx4 v251, v[152:155], s[44:45] offset:-4064\n"
    "v_exp_f32_e32 v105, v105\n"
    "v_exp_f32_e32 v106, v106\n"
    "v_mfma_f32_32x32x8_bf16 v[144:159], v[194:195], v[196:197], 0\n"
    "v_exp_f32_e32 v107, v107\n"
    "v_exp_f32_e32 v108, v108\n"
    "v_exp_f32_e32 v109, v109\n"
    "v_exp_f32_e32 v110, v110\n"
    "v_exp_f32_e64 v111, v111\n"
    "v_cvt_pk_bf16_f32 v104, v104, v105\n"
    "v_cvt_pk_bf16_f32 v105, v106, v107\n"
    "v_cvt_pk_bf16_f32 v106, v108, v109\n"
    "v_cvt_pk_bf16_f32 v107, v110, v111\n"
    "v_cvt_pk_bf16_f32 v204, v160, v161\n"
    "v_mfma_f32_32x32x16_bf16 v[144:159], v[72:75], v[96:99], v[144:159]\n"
    "ds_read_b128 v[216:219], v242 offset:28672\n"
    "v_cvt_pk_bf16_f32 v205, v162, v163\n"
    "v_cvt_pk_bf16_f32 v206, v164, v165\n"
    "v_cvt_pk_bf16_f32 v207, v166, v167\n"
    "v_cvt_pk_bf16_f32 v208, v168, v169\n"
    "v_cvt_pk_bf16_f32 v209, v170, v171\n"
    "s_waitcnt lgkmcnt(2)\n"
    "v_mfma_f32_16x16x32_bf16 v[184:187], v[232:235], v[176:179], 0\n"
    "ds_read_b128 v[220:223], v243 offset:0\n"
    "v_cvt_pk_bf16_f32 v210, v172, v173\n"
    "s_waitcnt lgkmcnt(2)\n"
    "v_mfma_f32_16x16x32_bf16 v[184:187], v[212:215], v[180:183], v[184:187]\n"
    "ds_read_b128 v[224:227], v242 offset:29696\n"
    "v_cvt_pk_bf16_f32 v211, v174, v175\n"
    "v_mfma_f32_32x32x16_bf16 v[144:159], v[76:79], v[104:107], v[144:159]\n"
    "ds_read_b128 v[228:231], v244 offset:0\n"
    "v_exp_f32_e32 v112, v112\n"
    "v_exp_f32_e32 v113, v113\n"
    "v_exp_f32_e64 v114, v114\n"
    "v_mfma_f32_16x16x32_bf16 v[188:191], v[236:239], v[104:107], v[188:191]\n"
    "ds_read_b128 v[232:235], v242 offset:30720\n"
    "v_exp_f32_e64 v115, v115\n"
    "v_mfma_f32_32x32x16_bf16 v[96:111], v[0:3], v[176:179], 0\n"
    "ds_read_b128 v[212:215], v245 offset:0\n"
    "v_exp_f32_e32 v116, v116\n"
    "v_exp_f32_e32 v117, v117\n"
    "v_exp_f32_e64 v118, v118\n"
    "v_mfma_f32_32x32x16_bf16 v[96:111], v[4:7], v[180:183], v[96:111]\n"
    "s_waitcnt lgkmcnt(5)\n"
    "v_exp_f32_e32 v119, v119\n"
    "v_cvt_pk_bf16_f32 v112, v112, v113\n"
    "v_cvt_pk_bf16_f32 v113, v114, v115\n"
    "v_cvt_pk_bf16_f32 v114, v116, v117\n"
    "v_cvt_pk_bf16_f32 v115, v118, v119\n"
    "s_waitcnt lgkmcnt(4)\n"
    "v_mfma_f32_32x32x16_bf16 v[160:175], v[216:219], v[220:223], 0\n"
    "ds_read_b128 v[216:219], v242 offset:31744\n"
    "v_exp_f32_e32 v120, v120\n"
    "v_exp_f32_e32 v121, v121\n"
    "v_exp_f32_e32 v122, v122\n"
    "v_mfma_f32_32x32x16_bf16 v[144:159], v[80:83], v[112:115], v[144:159]\n"
    "ds_read_b128 v[220:223], v246 offset:0\n"
    "v_exp_f32_e32 v123, v123\n"
    "v_exp_f32_e32 v124, v124\n"
    "v_exp_f32_e64 v125, v125\n"
    "v_mfma_f32_16x16x32_bf16 v[188:191], v[236:239], v[112:115], v[188:191]\n"
    "s_waitcnt lgkmcnt(2)\n"
    "v_exp_f32_e32 v126, v126\n"
    "v_mfma_f32_32x32x16_bf16 v[160:175], v[224:227], v[228:231], v[160:175]\n"
    "ds_read_b128 v[228:231], v242 offset:0\n"
    "v_exp_f32_e64 v127, v127\n"
    "v_cvt_pk_bf16_f32 v120, v120, v121\n"
    "v_cvt_pk_bf16_f32 v121, v122, v123\n"
    "v_cvt_pk_bf16_f32 v122, v124, v125\n"
    "v_mfma_f32_32x32x16_bf16 v[160:175], v[232:235], v[212:215], v[160:175]\n"
    "ds_read_b128 v[232:235], v243 offset:8192\n"
    "v_cvt_pk_bf16_f32 v123, v126, v127\n"
    "v_exp_f32_e32 v128, v128\n"
    "v_exp_f32_e32 v129, v129\n"
    "v_mfma_f32_32x32x16_bf16 v[144:159], v[84:87], v[120:123], v[144:159]\n"
    "ds_read_b128 v[212:215], v242 offset:1024\n"
    "v_exp_f32_e32 v130, v130\n"
    "v_exp_f32_e32 v131, v131\n"
    "v_exp_f32_e64 v132, v132\n"
    "v_mfma_f32_16x16x32_bf16 v[188:191], v[236:239], v[120:123], v[188:191]\n"
    "s_waitcnt lgkmcnt(3)\n"
    "v_exp_f32_e32 v133, v133\n"
    "v_mfma_f32_32x32x16_bf16 v[112:127], v[8:11], v[176:179], 0\n"
    "v_exp_f32_e32 v134, v134\n"
    "v_exp_f32_e32 v135, v135\n"
    "v_cvt_pk_bf16_f32 v128, v128, v129\n"
    "v_cvt_pk_bf16_f32 v129, v130, v131\n"
    "v_mfma_f32_32x32x16_bf16 v[112:127], v[12:15], v[180:183], v[112:127]\n"
    "s_waitcnt lgkmcnt(2)\n"
    "v_cvt_pk_bf16_f32 v130, v132, v133\n"
    "v_cvt_pk_bf16_f32 v131, v134, v135\n"
    "v_exp_f32_e32 v136, v136\n"
    "v_exp_f32_e64 v137, v137\n"
    "v_mfma_f32_32x32x16_bf16 v[160:175], v[216:219], v[220:223], v[160:175]\n"
    "s_cmp_lt_i32 s37, s36\n"
    "s_cbranch_scc0 .Lskip1_%=\n"
    "s_add_u32 s50, s40, 0\n"
    "s_addc_u32 s51, s41, 0\n"
    "s_add_u32 m0, s52, 0\n"
    "s_nop 0\n"
    "global_load_lds_dwordx4 v252, s[50:51]\n"
    "global_load_lds_dwordx4 v253, s[50:51] offset:1024\n"
    "global_load_lds_dwordx4 v252, s[50:51] offset:2048\n"
    "global_load_lds_dwordx4 v253, s[50:51] offset:3072\n"
    ".Lskip1_%=:\n"
    "v_exp_f32_e32 v138, v138\n"
    "v_exp_f32_e32 v139, v139\n"
    "v_mfma_f32_32x32x16_bf16 v[144:159], v[88:91], v[128:131], v[144:159]\n"
    "ds_read_b128 v[216:219], v244 offset:8192\n"
    "v_exp_f32_e32 v140, v140\n"
    "v_exp_f32_e32 v141, v141\n"
    "v_exp_f32_e64 v142, v142\n"
    "v_mfma_f32_16x16x32_bf16 v[188:191], v[236:239], v[128:131], v[188:191]\n"
    "ds_read_b128 v[220:223], v242 offset:2048\n"
    "v_exp_f32_e64 v143, v143\n"
    "v_cvt_pk_bf16_f32 v224, v136, v137\n"
    "v_cvt_pk_bf16_f32 v225, v138, v139\n"
    "v_cvt_pk_bf16_f32 v226, v140, v141\n"
    "v_cvt_pk_bf16_f32 v227, v142, v143\n"
    "v_mfma_f32_32x32x16_bf16 v[128:143], v[16:19], v[176:179], 0\n"
    "s_waitcnt lgkmcnt(2)\n"
    "v_max3_f32 v200, v96, v97, v98\n"
    "v_max3_f32 v200, v200, v99, v99\n"
    "v_max3_f32 v200, v200, v100, v101\n"
    "v_max3_f32 v200, v200, v102, v103\n"
    "v_max3_f32 v200, v200, v104, v105\n"
    "v_max3_f32 v200, v200, v106, v107\n"
    "v_mfma_f32_32x32x16_bf16 v[128:143], v[20:23], v[180:183], v[128:143]\n"
    "v_max3_f32 v200, v200, v108, v109\n"
    "v_max3_f32 v200, v200, v110, v111\n"
    "v_max3_f32 v200, v200, v112, v113\n"
    "v_max3_f32 v200, v200, v114, v115\n"
    "v_max3_f32 v200, v200, v116, v117\n"
    "v_max3_f32 v200, v200, v118, v119\n"
    "v_mfma_f32_32x32x16_bf16 v[144:159], v[92:95], v[224:227], v[144:159]\n"
    "v_max3_f32 v200, v200, v120, v121\n"
    "v_max3_f32 v200, v200, v122, v123\n"
    "v_max3_f32 v200, v200, v124, v125\n"
    "v_max3_f32 v200, v200, v126, v127\n"
    "v_max3_f32 v200, v200, v184, v185\n"
    "v_max3_f32 v200, v200, v128, v129\n"
    "v_mfma_f32_16x16x32_bf16 v[188:191], v[236:239], v[224:227], v[188:191]\n"
    "ds_read_b128 v[224:227], v245 offset:8192\n"
    "v_max3_f32 v200, v200, v130, v131\n"
    "v_max3_f32 v200, v200, v132, v133\n"
    "v_max3_f32 v200, v200, v134, v135\n"
    "v_max3_f32 v200, v200, v136, v137\n"
    "v_max3_f32 v200, v200, v138, v139\n"
    "v_max3_f32 v200, v200, v140, v141\n"
    "v_max3_f32 v200, v200, v142, v143\n"
    "v_mov_b32_e32 v201, v200\n"
    "v_rcp_f32_e32 v203, v188\n"
    "s_nop 0\n"
    "v_permlane32_swap_b32_e32 v200, v201\n"
    "v_max_f32_e32 v200, v200, v201\n"
    "v_and_b32_e32 v201, s59, v200\n"
    "v_sub_f32_e32 v201, v200, v201\n"
    "v_perm_b32 v198, v201, v200, s58\n"
    "v_and_b32_e32 v199, s59, v201\n"
    "v_sub_f32_e32 v199, v201, v199\n"
    "v_lshrrev_b32_e32 v199, 16, v199\n"
    "v_sub_f32_e32 v184, v184, v200\n"
    "v_sub_f32_e64 v185, v185, v200\n"
    "v_mfma_f32_32x32x8_bf16 v[96:111], v[240:241], v[198:199], v[96:111]\n"
    "v_exp_f32_e32 v184, v184\n"
    "v_exp_f32_e32 v185, v185\n"
    "v_fmac_f32_e32 v160, v144, v203\n"
    "v_fmac_f32_e32 v161, v145, v203\n"
    "v_fmac_f32_e64 v162, v146, v203\n"
    "v_mfma_f32_32x32x8_bf16 v[112:127], v[240:241], v[198:199], v[112:127]\n"
    "v_fmac_f32_e32 v163, v147, v203\n"
    "v_fmac_f32_e32 v164, v148, v203\n"
    "v_fmac_f32_e32 v165, v149, v203\n"
    "v_fmac_f32_e32 v166, v150, v203\n"
    "v_fmac_f32_e32 v167, v151, v203\n"
    "v_fmac_f32_e32 v168, v152, v203\n"
    "v_fmac_f32_e64 v169, v153, v203\n"
    "v_mfma_f32_32x32x8_bf16 v[128:143], v[240:241], v[198:199], v[128:143]\n"
    "v_fmac_f32_e32 v170, v154, v203\n"
    "v_fmac_f32_e32 v171, v155, v203\n"
    "v_fmac_f32_e32 v172, v156, v203\n"
    "v_fmac_f32_e32 v173, v157, v203\n"
    "v_fmac_f32_e32 v174, v158, v203\n"
    "v_fmac_f32_e32 v175, v159, v203\n"
    "v_cvt_pk_bf16_f32 v160, v160, v161\n"
    "v_mfma_f32_32x32x16_bf16 v[144:159], v[228:231], v[232:235], 0\n"
    "ds_read_b128 v[228:231], v242 offset:3072\n"
    "v_cvt_pk_bf16_f32 v161, v162, v163\n"
    "v_cvt_pk_bf16_f32 v162, v164, v165\n"
    "v_cvt_pk_bf16_f32 v163, v166, v167\n"
    "v_pk_max_i16 v160, v160, 0\n"
    "v_pk_max_i16 v161, v161, 0\n"
    "s_waitcnt lgkmcnt(1)\n"
    "v_mfma_f32_32x32x16_bf16 v[144:159], v[212:215], v[216:219], v[144:159]\n"
    "ds_read_b128 v[232:235], v246 offset:8192\n"
    "v_pk_max_i16 v162, v162, 0\n"
    "v_pk_max_i16 v163, v163, 0\n"
    "v_cvt_pk_bf16_f32 v168, v168, v169\n"
    "v_cvt_pk_bf16_f32 v169, v170, v171\n"
    "v_cvt_pk_bf16_f32 v170, v172, v173\n"
    "v_mfma_f32_32x32x16_bf16 v[144:159], v[220:223], v[224:227], v[144:159]\n"
    "global_store_dwordx4 v251, v[160:163], s[44:45] offset:-4032\n"
    "v_cvt_pk_bf16_f32 v171, v174, v175\n"
    "v_pk_max_i16 v168, v168, 0\n"
    "v_pk_max_i16 v169, v169, 0\n"
    "v_pk_max_i16 v170, v170, 0\n"
    "v_pk_max_i16 v171, v171, 0\n"
    "v_cvt_pk_bf16_f32 v196, v184, v185\n"
    "v_exp_f32_e32 v96, v96\n"
    "v_exp_f32_e64 v97, v97\n"
    "v_mfma_f32_16x16x16_bf16 v[188:191], v[236:237], v[196:197], 0\n"
    "v_exp_f32_e32 v98, v98\n"
    "v_exp_f32_e32 v99, v99\n"
    "v_exp_f32_e32 v100, v100\n"
    "v_exp_f32_e32 v101, v101\n"
    "v_exp_f32_e32 v102, v102\n"
    "v_exp_f32_e32 v103, v103\n"
    "v_cvt_pk_bf16_f32 v96, v96, v97\n"
    "v_cvt_pk_bf16_f32 v97, v98, v99\n"
    "v_cvt_pk_bf16_f32 v98, v100, v101\n"
    "s_waitcnt lgkmcnt(0)\n"
    "v_mfma_f32_32x32x16_bf16 v[144:159], v[228:231], v[232:235], v[144:159]\n"
    "ds_read_b128 v[212:215], v249 offset:576\n"
    "v_cvt_pk_bf16_f32 v99, v102, v103\n"
    "ds_read_b128 v[216:219], v249 offset:608\n"
    "v_exp_f32_e32 v104, v104\n"
    "v_mfma_f32_16x16x32_bf16 v[188:191], v[236:239], v[96:99], v[188:191]\n"
    "global_store_dwordx4 v251, v[168:171], s[44:45] offset:-4000\n"
    "v_exp_f32_e32 v105, v105\n"
    "v_exp_f32_e32 v106, v106\n"
    "v_mfma_f32_32x32x8_bf16 v[160:175], v[192:193], v[196:197], 0\n"
    "v_exp_f32_e32 v107, v107\n"
    "v_exp_f32_e32 v108, v108\n"
    "v_exp_f32_e32 v109, v109\n"
    "v_exp_f32_e32 v110, v110\n"
    "v_exp_f32_e64 v111, v111\n"
    "v_cvt_pk_bf16_f32 v104, v104, v105\n"
    "v_cvt_pk_bf16_f32 v105, v106, v107\n"
    "v_cvt_pk_bf16_f32 v106, v108, v109\n"
    "v_cvt_pk_bf16_f32 v107, v110, v111\n"
    "v_cvt_pk_bf16_f32 v176, v144, v145\n"
    "v_mfma_f32_32x32x16_bf16 v[160:175], v[48:51], v[96:99], v[160:175]\n"
    "ds_read_b128 v[220:223], v242 offset:24576\n"
    "v_cvt_pk_bf16_f32 v177, v146, v147\n"
    "v_cvt_pk_bf16_f32 v178, v148, v149\n"
    "v_cvt_pk_bf16_f32 v179, v150, v151\n"
    "v_cvt_pk_bf16_f32 v180, v152, v153\n"
    "v_cvt_pk_bf16_f32 v181, v154, v155\n"
    "s_waitcnt lgkmcnt(2)\n"
    "v_mfma_f32_16x16x32_bf16 v[184:187], v[212:215], v[204:207], 0\n"
    "ds_read_b128 v[224:227], v243 offset:4096\n"
    "v_cvt_pk_bf16_f32 v182, v156, v157\n"
    "s_waitcnt lgkmcnt(2)\n"
    "v_mfma_f32_16x16x32_bf16 v[184:187], v[216:219], v[208:211], v[184:187]\n"
    "ds_read_b128 v[228:231], v242 offset:25600\n"
    "v_cvt_pk_bf16_f32 v183, v158, v159\n"
    "v_mfma_f32_32x32x16_bf16 v[160:175], v[52:55], v[104:107], v[160:175]\n"
    "ds_read_b128 v[232:235], v244 offset:4096\n"
    "v_exp_f32_e32 v112, v112\n"
    "v_exp_f32_e32 v113, v113\n"
    "v_exp_f32_e64 v114, v114\n"
    "v_mfma_f32_16x16x32_bf16 v[188:191], v[236:239], v[104:107], v[188:191]\n"
    "ds_read_b128 v[212:215], v242 offset:26624\n"
    "v_exp_f32_e64 v115, v115\n"
    "v_mfma_f32_32x32x16_bf16 v[96:111], v[24:27], v[204:207], 0\n"
    "ds_read_b128 v[216:219], v245 offset:4096\n"
    "v_exp_f32_e32 v116, v116\n"
    "v_exp_f32_e32 v117, v117\n"
    "v_exp_f32_e64 v118, v118\n"
    "v_mfma_f32_32x32x16_bf16 v[96:111], v[28:31], v[208:211], v[96:111]\n"
    "s_waitcnt lgkmcnt(5)\n"
    "v_exp_f32_e32 v119, v119\n"
    "v_cvt_pk_bf16_f32 v112, v112, v113\n"
    "v_cvt_pk_bf16_f32 v113, v114, v115\n"
    "v_cvt_pk_bf16_f32 v114, v116, v117\n"
    "v_cvt_pk_bf16_f32 v115, v118, v119\n"
    "s_waitcnt lgkmcnt(4)\n"
    "v_mfma_f32_32x32x16_bf16 v[144:159], v[220:223], v[224:227], 0\n"
    "ds_read_b128 v[220:223], v242 offset:27648\n"
    "v_exp_f32_e32 v120, v120\n"
    "v_exp_f32_e32 v121, v121\n"
    "v_exp_f32_e32 v122, v122\n"
    "v_mfma_f32_32x32x16_bf16 v[160:175], v[56:59], v[112:115], v[160:175]\n"
    "ds_read_b128 v[224:227], v246 offset:4096\n"
    "v_exp_f32_e32 v123, v123\n"
    "v_exp_f32_e32 v124, v124\n"
    "v_exp_f32_e64 v125, v125\n"
    "v_mfma_f32_16x16x32_bf16 v[188:191], v[236:239], v[112:115], v[188:191]\n"
    "s_waitcnt lgkmcnt(2)\n"
    "v_exp_f32_e32 v126, v126\n"
    "v_mfma_f32_32x32x16_bf16 v[144:159], v[228:231], v[232:235], v[144:159]\n"
    "ds_read_b128 v[232:235], v242 offset:4096\n"
    "v_exp_f32_e64 v127, v127\n"
    "v_cvt_pk_bf16_f32 v120, v120, v121\n"
    "v_cvt_pk_bf16_f32 v121, v122, v123\n"
    "v_cvt_pk_bf16_f32 v122, v124, v125\n"
    "v_mfma_f32_32x32x16_bf16 v[144:159], v[212:215], v[216:219], v[144:159]\n"
    "ds_read_b128 v[212:215], v243 offset:8192\n"
    "v_cvt_pk_bf16_f32 v123, v126, v127\n"
    "v_exp_f32_e32 v128, v128\n"
    "v_exp_f32_e32 v129, v129\n"
    "v_mfma_f32_32x32x16_bf16 v[160:175], v[60:63], v[120:123], v[160:175]\n"
    "ds_read_b128 v[216:219], v242 offset:5120\n"
    "v_exp_f32_e32 v130, v130\n"
    "v_exp_f32_e32 v131, v131\n"
    "v_exp_f32_e64 v132, v132\n"
    "v_mfma_f32_16x16x32_bf16 v[188:191], v[236:239], v[120:123], v[188:191]\n"
    "s_waitcnt lgkmcnt(3)\n"
    "v_exp_f32_e32 v133, v133\n"
    "v_mfma_f32_32x32x16_bf16 v[112:127], v[32:35], v[204:207], 0\n"
    "v_exp_f32_e32 v134, v134\n"
    "v_exp_f32_e32 v135, v135\n"
    "v_cvt_pk_bf16_f32 v128, v128, v129\n"
    "v_cvt_pk_bf16_f32 v129, v130, v131\n"
    "v_mfma_f32_32x32x16_bf16 v[112:127], v[36:39], v[208:211], v[112:127]\n"
    "s_waitcnt lgkmcnt(2)\n"
    "v_cvt_pk_bf16_f32 v130, v132, v133\n"
    "v_cvt_pk_bf16_f32 v131, v134, v135\n"
    "v_exp_f32_e32 v136, v136\n"
    "v_exp_f32_e64 v137, v137\n"
    "v_mfma_f32_32x32x16_bf16 v[144:159], v[220:223], v[224:227], v[144:159]\n"
    "ds_read_b128 v[220:223], v244 offset:8192\n"
    "v_exp_f32_e32 v138, v138\n"
    "v_exp_f32_e32 v139, v139\n"
    "v_exp_f32_e64 v140, v140\n"
    "v_mfma_f32_32x32x16_bf16 v[160:175], v[64:67], v[128:131], v[160:175]\n"
    "ds_read_b128 v[224:227], v242 offset:6144\n"
    "v_exp_f32_e32 v141, v141\n"
    "v_exp_f32_e32 v142, v142\n"
    "v_exp_f32_e64 v143, v143\n"
    "v_mfma_f32_16x16x32_bf16 v[188:191], v[236:239], v[128:131], v[188:191]\n"
    "s_waitcnt lgkmcnt(2)\n"
    "v_cvt_pk_bf16_f32 v228, v136, v137\n"
    "v_cvt_pk_bf16_f32 v229, v138, v139\n"
    "v_cvt_pk_bf16_f32 v230, v140, v141\n"
    "v_cvt_pk_bf16_f32 v231, v142, v143\n"
    "v_mfma_f32_32x32x16_bf16 v[128:143], v[40:43], v[204:207], 0\n"
    "v_max3_f32 v200, v96, v97, v98\n"
    "v_max3_f32 v200, v200, v99, v99\n"
    "v_max3_f32 v200, v200, v100, v101\n"
    "v_max3_f32 v200, v200, v102, v103\n"
    "v_max3_f32 v200, v200, v104, v105\n"
    "v_max3_f32 v200, v200, v106, v107\n"
    "v_mfma_f32_32x32x16_bf16 v[128:143], v[44:47], v[208:211], v[128:143]\n"
    "v_max3_f32 v200, v200, v108, v109\n"
    "v_max3_f32 v200, v200, v110, v111\n"
    "v_max3_f32 v200, v200, v112, v113\n"
    "v_max3_f32 v200, v200, v114, v115\n"
    "v_max3_f32 v200, v200, v116, v117\n"
    "v_max3_f32 v200, v200, v118, v119\n"
    "v_mfma_f32_32x32x16_bf16 v[160:175], v[68:71], v[228:231], v[160:175]\n"
    "s_waitcnt lgkmcnt(1)\n"
    "v_max3_f32 v200, v200, v120, v121\n"
    "v_max3_f32 v200, v200, v122, v123\n"
    "v_max3_f32 v200, v200, v124, v125\n"
    "v_max3_f32 v200, v200, v126, v127\n"
    "v_max3_f32 v200, v200, v184, v185\n"
    "v_max3_f32 v200, v200, v128, v129\n"
    "v_mfma_f32_16x16x32_bf16 v[188:191], v[236:239], v[228:231], v[188:191]\n"
    "ds_read_b128 v[228:231], v245 offset:8192\n"
    "v_max3_f32 v200, v200, v130, v131\n"
    "v_max3_f32 v200, v200, v132, v133\n"
    "v_max3_f32 v200, v200, v134, v135\n"
    "v_max3_f32 v200, v200, v136, v137\n"
    "v_max3_f32 v200, v200, v138, v139\n"
    "v_max3_f32 v200, v200, v140, v141\n"
    "v_max3_f32 v200, v200, v142, v143\n"
    "v_mov_b32_e32 v201, v200\n"
    "v_rcp_f32_e32 v203, v188\n"
    "s_nop 0\n"
    "v_permlane32_swap_b32_e32 v200, v201\n"
    "v_max_f32_e32 v200, v200, v201\n"
    "v_and_b32_e32 v201, s59, v200\n"
    "v_sub_f32_e64 v201, v200, v201\n"
    "v_perm_b32 v198, v201, v200, s58\n"
    "v_and_b32_e32 v199, s59, v201\n"
    "v_sub_f32_e32 v199, v201, v199\n"
    "v_lshrrev_b32_e32 v199, 16, v199\n"
    "v_sub_f32_e32 v184, v184, v200\n"
    "v_sub_f32_e64 v185, v185, v200\n"
    "v_mfma_f32_32x32x8_bf16 v[96:111], v[240:241], v[198:199], v[96:111]\n"
    "v_exp_f32_e32 v184, v184\n"
    "v_exp_f32_e32 v185, v185\n"
    "v_fmac_f32_e32 v144, v160, v203\n"
    "v_fmac_f32_e32 v145, v161, v203\n"
    "v_fmac_f32_e64 v146, v162, v203\n"
    "v_mfma_f32_32x32x8_bf16 v[112:127], v[240:241], v[198:199], v[112:127]\n"
    "v_fmac_f32_e32 v147, v163, v203\n"
    "v_fmac_f32_e32 v148, v164, v203\n"
    "v_fmac_f32_e32 v149, v165, v203\n"
    "v_fmac_f32_e32 v150, v166, v203\n"
    "v_fmac_f32_e32 v151, v167, v203\n"
    "v_fmac_f32_e32 v152, v168, v203\n"
    "v_fmac_f32_e64 v153, v169, v203\n"
    "v_mfma_f32_32x32x8_bf16 v[128:143], v[240:241], v[198:199], v[128:143]\n"
    "v_fmac_f32_e32 v154, v170, v203\n"
    "v_fmac_f32_e32 v155, v171, v203\n"
    "v_fmac_f32_e32 v156, v172, v203\n"
    "v_fmac_f32_e32 v157, v173, v203\n"
    "v_fmac_f32_e32 v158, v174, v203\n"
    "v_fmac_f32_e32 v159, v175, v203\n"
    "v_cvt_pk_bf16_f32 v144, v144, v145\n"
    "v_mfma_f32_32x32x16_bf16 v[160:175], v[232:235], v[212:215], 0\n"
    "ds_read_b128 v[232:235], v242 offset:7168\n"
    "v_cvt_pk_bf16_f32 v145, v146, v147\n"
    "v_cvt_pk_bf16_f32 v146, v148, v149\n"
    "v_cvt_pk_bf16_f32 v147, v150, v151\n"
    "v_pk_max_i16 v144, v144, 0\n"
    "v_pk_max_i16 v145, v145, 0\n"
    "v_mfma_f32_32x32x16_bf16 v[160:175], v[216:219], v[220:223], v[160:175]\n"
    "ds_read_b128 v[212:215], v246 offset:8192\n"
    "v_pk_max_i16 v146, v146, 0\n"
    "v_pk_max_i16 v147, v147, 0\n"
    "v_cvt_pk_bf16_f32 v152, v152, v153\n"
    "v_cvt_pk_bf16_f32 v153, v154, v155\n"
    "v_cvt_pk_bf16_f32 v154, v156, v157\n"
    "s_waitcnt lgkmcnt(2)\n"
    "v_mfma_f32_32x32x16_bf16 v[160:175], v[224:227], v[228:231], v[160:175]\n"
    "global_store_dwordx4 v251, v[144:147], s[44:45] offset:0\n"
    "v_cvt_pk_bf16_f32 v155, v158, v159\n"
    "v_pk_max_i16 v152, v152, 0\n"
    "v_pk_max_i16 v153, v153, 0\n"
    "v_pk_max_i16 v154, v154, 0\n"
    "v_pk_max_i16 v155, v155, 0\n"
    "v_cvt_pk_bf16_f32 v196, v184, v185\n"
    "v_exp_f32_e32 v96, v96\n"
    "v_exp_f32_e64 v97, v97\n"
    "v_mfma_f32_16x16x16_bf16 v[188:191], v[236:237], v[196:197], 0\n"
    "v_exp_f32_e32 v98, v98\n"
    "v_exp_f32_e32 v99, v99\n"
    "v_exp_f32_e32 v100, v100\n"
    "v_exp_f32_e32 v101, v101\n"
    "v_exp_f32_e32 v102, v102\n"
    "v_exp_f32_e32 v103, v103\n"
    "v_cvt_pk_bf16_f32 v96, v96, v97\n"
    "v_cvt_pk_bf16_f32 v97, v98, v99\n"
    "s_waitcnt lgkmcnt(0)\n"
    "v_mfma_f32_32x32x16_bf16 v[160:175], v[232:235], v[212:215], v[160:175]\n"
    "ds_read_b128 v[216:219], v249 offset:512\n"
    "v_cvt_pk_bf16_f32 v98, v100, v101\n"
    "v_cvt_pk_bf16_f32 v99, v102, v103\n"
    "ds_read_b128 v[220:223], v249 offset:544\n"
    "v_exp_f32_e32 v104, v104\n"
    "v_mfma_f32_16x16x32_bf16 v[188:191], v[236:239], v[96:99], v[188:191]\n"
    "global_store_dwordx4 v251, v[152:155], s[44:45] offset:32\n"
    "v_exp_f32_e32 v105, v105\n"
    "v_exp_f32_e32 v106, v106\n"
    "v_mfma_f32_32x32x8_bf16 v[144:159], v[194:195], v[196:197], 0\n"
    "v_exp_f32_e32 v107, v107\n"
    "v_exp_f32_e32 v108, v108\n"
    "v_exp_f32_e32 v109, v109\n"
    "v_exp_f32_e32 v110, v110\n"
    "v_exp_f32_e64 v111, v111\n"
    "v_cvt_pk_bf16_f32 v104, v104, v105\n"
    "v_cvt_pk_bf16_f32 v105, v106, v107\n"
    "v_cvt_pk_bf16_f32 v106, v108, v109\n"
    "v_cvt_pk_bf16_f32 v107, v110, v111\n"
    "v_cvt_pk_bf16_f32 v204, v160, v161\n"
    "v_mfma_f32_32x32x16_bf16 v[144:159], v[72:75], v[96:99], v[144:159]\n"
    "ds_read_b128 v[224:227], v242 offset:28672\n"
    "v_cvt_pk_bf16_f32 v205, v162, v163\n"
    "v_cvt_pk_bf16_f32 v206, v164, v165\n"
    "v_cvt_pk_bf16_f32 v207, v166, v167\n"
    "v_cvt_pk_bf16_f32 v208, v168, v169\n"
    "v_cvt_pk_bf16_f32 v209, v170, v171\n"
    "s_waitcnt lgkmcnt(2)\n"
    "v_mfma_f32_16x16x32_bf16 v[184:187], v[216:219], v[176:179], 0\n"
    "ds_read_b128 v[228:231], v243 offset:4096\n"
    "v_cvt_pk_bf16_f32 v210, v172, v173\n"
    "s_waitcnt lgkmcnt(2)\n"
    "v_mfma_f32_16x16x32_bf16 v[184:187], v[220:223], v[180:183], v[184:187]\n"
    "ds_read_b128 v[232:235], v242 offset:29696\n"
    "v_cvt_pk_bf16_f32 v211, v174, v175\n"
    "v_mfma_f32_32x32x16_bf16 v[144:159], v[76:79], v[104:107], v[144:159]\n"
    "ds_read_b128 v[212:215], v244 offset:4096\n"
    "v_exp_f32_e32 v112, v112\n"
    "v_exp_f32_e32 v113, v113\n"
    "v_exp_f32_e64 v114, v114\n"
    "v_mfma_f32_16x16x32_bf16 v[188:191], v[236:239], v[104:107], v[188:191]\n"
    "ds_read_b128 v[216:219], v242 offset:30720\n"
    "v_exp_f32_e64 v115, v115\n"
    "v_mfma_f32_32x32x16_bf16 v[96:111], v[0:3], v[176:179], 0\n"
    "ds_read_b128 v[220:223], v245 offset:4096\n"
    "v_exp_f32_e32 v116, v116\n"
    "v_exp_f32_e32 v117, v117\n"
    "v_exp_f32_e64 v118, v118\n"
    "v_mfma_f32_32x32x16_bf16 v[96:111], v[4:7], v[180:183], v[96:111]\n"
    "s_waitcnt lgkmcnt(5)\n"
    "v_exp_f32_e32 v119, v119\n"
    "v_cvt_pk_bf16_f32 v112, v112, v113\n"
    "v_cvt_pk_bf16_f32 v113, v114, v115\n"
    "v_cvt_pk_bf16_f32 v114, v116, v117\n"
    "v_cvt_pk_bf16_f32 v115, v118, v119\n"
    "s_waitcnt lgkmcnt(4)\n"
    "v_mfma_f32_32x32x16_bf16 v[160:175], v[224:227], v[228:231], 0\n"
    "ds_read_b128 v[224:227], v242 offset:31744\n"
    "v_exp_f32_e32 v120, v120\n"
    "v_exp_f32_e32 v121, v121\n"
    "v_exp_f32_e32 v122, v122\n"
    "v_mfma_f32_32x32x16_bf16 v[144:159], v[80:83], v[112:115], v[144:159]\n"
    "ds_read_b128 v[228:231], v246 offset:4096\n"
    "v_exp_f32_e32 v123, v123\n"
    "v_exp_f32_e32 v124, v124\n"
    "v_exp_f32_e64 v125, v125\n"
    "v_mfma_f32_16x16x32_bf16 v[188:191], v[236:239], v[112:115], v[188:191]\n"
    "s_waitcnt lgkmcnt(2)\n"
    "v_exp_f32_e32 v126, v126\n"
    "v_mfma_f32_32x32x16_bf16 v[160:175], v[232:235], v[212:215], v[160:175]\n"
    "ds_read_b128 v[212:215], v249 offset:576\n"
    "v_exp_f32_e64 v127, v127\n"
    "v_cvt_pk_bf16_f32 v120, v120, v121\n"
    "v_cvt_pk_bf16_f32 v121, v122, v123\n"
    "v_cvt_pk_bf16_f32 v122, v124, v125\n"
    "v_mfma_f32_32x32x16_bf16 v[160:175], v[216:219], v[220:223], v[160:175]\n"
    "ds_read_b128 v[216:219], v249 offset:608\n"
    "v_cvt_pk_bf16_f32 v123, v126, v127\n"
    "v_exp_f32_e32 v128, v128\n"
    "v_exp_f32_e32 v129, v129\n"
    "v_mfma_f32_32x32x16_bf16 v[144:159], v[84:87], v[120:123], v[144:159]\n"
    "ds_read_b128 v[220:223], v242 offset:24576\n"
    "v_exp_f32_e32 v130, v130\n"
    "v_exp_f32_e32 v131, v131\n"
    "v_exp_f32_e64 v132, v132\n"
    "v_mfma_f32_16x16x32_bf16 v[188:191], v[236:239], v[120:123], v[188:191]\n"
    "s_waitcnt lgkmcnt(3)\n"
    "v_exp_f32_e32 v133, v133\n"
    "v_mfma_f32_32x32x16_bf16 v[112:127], v[8:11], v[176:179], 0\n"
    "v_exp_f32_e32 v134, v134\n"
    "v_exp_f32_e32 v135, v135\n"
    "v_cvt_pk_bf16_f32 v128, v128, v129\n"
    "v_cvt_pk_bf16_f32 v129, v130, v131\n"
    "v_mfma_f32_32x32x16_bf16 v[112:127], v[12:15], v[180:183], v[112:127]\n"
    "s_waitcnt lgkmcnt(2)\n"
    "v_cvt_pk_bf16_f32 v130, v132, v133\n"
    "v_cvt_pk_bf16_f32 v131, v134, v135\n"
    "v_exp_f32_e32 v136, v136\n"
    "v_exp_f32_e64 v137, v137\n"
    "v_mfma_f32_32x32x16_bf16 v[160:175], v[224:227], v[228:231], v[160:175]\n"
    "s_cmp_lt_i32 s37, s36\n"
    "s_cbranch_scc0 .Lskip2_%=\n"
    "s_add_u32 s50, s40, 4096\n"
    "s_addc_u32 s51, s41, 0\n"
    "s_add_u32 m0, s52, 4096\n"
    "s_nop 0\n"
    "global_load_lds_dwordx4 v252, s[50:51]\n"
    "global_load_lds_dwordx4 v253, s[50:51] offset:1024\n"
    "global_load_lds_dwordx4 v252, s[50:51] offset:2048\n"
    "global_load_lds_dwordx4 v253, s[50:51] offset:3072\n"
    ".Lskip2_%=:\n"
    "v_exp_f32_e32 v138, v138\n"
    "v_exp_f32_e32 v139, v139\n"
    "v_mfma_f32_32x32x16_bf16 v[144:159], v[88:91], v[128:131], v[144:159]\n"
    "ds_read_b128 v[224:227], v243 offset:8192\n"
    "v_exp_f32_e32 v140, v140\n"
    "v_exp_f32_e32 v141, v141\n"
    "v_exp_f32_e64 v142, v142\n"
    "v_mfma_f32_16x16x32_bf16 v[188:191], v[236:239], v[128:131], v[188:191]\n"
    "ds_read_b128 v[228:231], v242 offset:25600\n"
    "v_exp_f32_e64 v143, v143\n"
    "v_cvt_pk_bf16_f32 v232, v136, v137\n"
    "v_cvt_pk_bf16_f32 v233, v138, v139\n"
    "v_cvt_pk_bf16_f32 v234, v140, v141\n"
    "v_cvt_pk_bf16_f32 v235, v142, v143\n"
    "v_mfma_f32_32x32x16_bf16 v[128:143], v[16:19], v[176:179], 0\n"
    "s_waitcnt lgkmcnt(2)\n"
    "v_max3_f32 v200, v96, v97, v98\n"
    "v_max3_f32 v200, v200, v99, v99\n"
    "v_max3_f32 v200, v200, v100, v101\n"
    "v_max3_f32 v200, v200, v102, v103\n"
    "v_max3_f32 v200, v200, v104, v105\n"
    "v_max3_f32 v200, v200, v106, v107\n"
    "v_mfma_f32_32x32x16_bf16 v[128:143], v[20:23], v[180:183], v[128:143]\n"
    "v_max3_f32 v200, v200, v108, v109\n"
    "v_max3_f32 v200, v200, v110, v111\n"
    "v_max3_f32 v200, v200, v112, v113\n"
    "v_max3_f32 v200, v200, v114, v115\n"
    "v_max3_f32 v200, v200, v116, v117\n"
    "v_max3_f32 v200, v200, v118, v119\n"
    "v_mfma_f32_32x32x16_bf16 v[144:159], v[92:95], v[232:235], v[144:159]\n"
    "v_max3_f32 v200, v200, v120, v121\n"
    "v_max3_f32 v200, v200, v122, v123\n"
    "v_max3_f32 v200, v200, v124, v125\n"
    "v_max3_f32 v200, v200, v126, v127\n"
    "v_max3_f32 v200, v200, v184, v185\n"
    "v_max3_f32 v200, v200, v128, v129\n"
    "v_mfma_f32_16x16x32_bf16 v[188:191], v[236:239], v[232:235], v[188:191]\n"
    "ds_read_b128 v[232:235], v244 offset:8192\n"
    "v_max3_f32 v200, v200, v130, v131\n"
    "v_max3_f32 v200, v200, v132, v133\n"
    "v_max3_f32 v200, v200, v134, v135\n"
    "v_max3_f32 v200, v200, v136, v137\n"
    "v_max3_f32 v200, v200, v138, v139\n"
    "v_max3_f32 v200, v200, v140, v141\n"
    "v_max3_f32 v200, v200, v142, v143\n"
    "v_mov_b32_e32 v201, v200\n"
    "v_rcp_f32_e32 v203, v188\n"
    "s_nop 0\n"
    "v_permlane32_swap_b32_e32 v200, v201\n"
    "v_max_f32_e32 v200, v200, v201\n"
    "v_and_b32_e32 v201, s59, v200\n"
    "v_sub_f32_e32 v201, v200, v201\n"
    "v_perm_b32 v198, v201, v200, s58\n"
    "v_and_b32_e32 v199, s59, v201\n"
    "v_sub_f32_e32 v199, v201, v199\n"
    "v_lshrrev_b32_e32 v199, 16, v199\n"
    "v_sub_f32_e32 v184, v184, v200\n"
    "v_sub_f32_e64 v185, v185, v200\n"
    "v_mfma_f32_32x32x8_bf16 v[96:111], v[240:241], v[198:199], v[96:111]\n"
    "v_exp_f32_e32 v184, v184\n"
    "v_exp_f32_e32 v185, v185\n"
    "v_fmac_f32_e32 v160, v144, v203\n"
    "v_fmac_f32_e32 v161, v145, v203\n"
    "v_fmac_f32_e64 v162, v146, v203\n"
    "v_mfma_f32_32x32x8_bf16 v[112:127], v[240:241], v[198:199], v[112:127]\n"
    "v_fmac_f32_e32 v163, v147, v203\n"
    "v_fmac_f32_e32 v164, v148, v203\n"
    "v_fmac_f32_e32 v165, v149, v203\n"
    "v_fmac_f32_e32 v166, v150, v203\n"
    "v_fmac_f32_e32 v167, v151, v203\n"
    "v_fmac_f32_e32 v168, v152, v203\n"
    "v_fmac_f32_e64 v169, v153, v203\n"
    "v_mfma_f32_32x32x8_bf16 v[128:143], v[240:241], v[198:199], v[128:143]\n"
    "v_fmac_f32_e32 v170, v154, v203\n"
    "v_fmac_f32_e32 v171, v155, v203\n"
    "v_fmac_f32_e32 v172, v156, v203\n"
    "v_fmac_f32_e32 v173, v157, v203\n"
    "v_fmac_f32_e32 v174, v158, v203\n"
    "v_fmac_f32_e32 v175, v159, v203\n"
    "v_cvt_pk_bf16_f32 v160, v160, v161\n"
    "s_waitcnt lgkmcnt(0)\n"
    "v_mfma_f32_32x32x16_bf16 v[144:159], v[220:223], v[224:227], 0\n"
    "ds_read_b128 v[220:223], v242 offset:27648\n"
    "v_cvt_pk_bf16_f32 v161, v162, v163\n"
    "v_cvt_pk_bf16_f32 v162, v164, v165\n"
    "v_cvt_pk_bf16_f32 v163, v166, v167\n"
    "v_pk_max_i16 v160, v160, 0\n"
    "v_pk_max_i16 v161, v161, 0\n"
    "v_mfma_f32_32x32x16_bf16 v[144:159], v[228:231], v[232:235], v[144:159]\n"
    "ds_read_b128 v[224:227], v246 offset:8192\n"
    "v_pk_max_i16 v162, v162, 0\n"
    "v_pk_max_i16 v163, v163, 0\n"
    "v_cvt_pk_bf16_f32 v168, v168, v169\n"
    "v_cvt_pk_bf16_f32 v169, v170, v171\n"
    "v_cvt_pk_bf16_f32 v170, v172, v173\n"
    "v_cvt_pk_bf16_f32 v171, v174, v175\n"
    "v_pk_max_i16 v168, v168, 0\n"
    "v_pk_max_i16 v169, v169, 0\n"
    "v_pk_max_i16 v170, v170, 0\n"
    "v_pk_max_i16 v171, v171, 0\n"
    "v_cvt_pk_bf16_f32 v196, v184, v185\n"
    "v_mfma_f32_16x16x32_bf16 v[184:187], v[212:215], v[204:207], 0\n"
    "ds_read_b128 v[212:215], v242 offset:26624\n"
    "v_exp_f32_e32 v96, v96\n"
    "v_mfma_f32_16x16x16_bf16 v[188:191], v[236:237], v[196:197], 0\n"
    "v_exp_f32_e32 v97, v97\n"
    "v_exp_f32_e32 v98, v98\n"
    "v_mfma_f32_16x16x32_bf16 v[184:187], v[216:219], v[208:211], v[184:187]\n"
    "ds_read_b128 v[216:219], v245 offset:8192\n"
    "v_exp_f32_e32 v99, v99\n"
    "v_exp_f32_e32 v100, v100\n"
    "v_exp_f32_e32 v101, v101\n"
    "v_exp_f32_e32 v102, v102\n"
    "v_exp_f32_e64 v103, v103\n"
    "v_cvt_pk_bf16_f32 v96, v96, v97\n"
    "v_cvt_pk_bf16_f32 v97, v98, v99\n"
    "v_cvt_pk_bf16_f32 v98, v100, v101\n"
    "v_cvt_pk_bf16_f32 v99, v102, v103\n"
    "global_store_dwordx4 v251, v[160:163], s[44:45] offset:64\n"
    "v_exp_f32_e64 v104, v104\n"
    "v_mfma_f32_16x16x32_bf16 v[188:191], v[236:239], v[96:99], v[188:191]\n"
    "global_store_dwordx4 v251, v[168:171], s[44:45] offset:96\n"
    "v_exp_f32_e32 v105, v105\n"
    "v_exp_f32_e32 v106, v106\n"
    "v_mfma_f32_32x32x8_bf16 v[160:175], v[192:193], v[196:197], 0\n"
    "v_exp_f32_e32 v107, v107\n"
    "v_exp_f32_e32 v108, v108\n"
    "v_exp_f32_e32 v109, v109\n"
    "v_exp_f32_e64 v110, v110\n"
    "s_waitcnt lgkmcnt(0)\n"
    "v_mfma_f32_32x32x16_bf16 v[144:159], v[212:215], v[216:219], v[144:159]\n"
    "ds_read_b128 v[232:235], v242 offset:28672\n"
    "v_exp_f32_e64 v111, v111\n"
    "v_cvt_pk_bf16_f32 v104, v104, v105\n"
    "v_cvt_pk_bf16_f32 v105, v106, v107\n"
    "v_cvt_pk_bf16_f32 v106, v108, v109\n"
    "v_mfma_f32_32x32x16_bf16 v[144:159], v[220:223], v[224:227], v[144:159]\n"
    "ds_read_b128 v[212:215], v243 offset:8192\n"
    "v_cvt_pk_bf16_f32 v107, v110, v111\n"
    "v_exp_f32_e32 v112, v112\n"
    "v_exp_f32_e32 v113, v113\n"
    "v_mfma_f32_32x32x16_bf16 v[160:175], v[48:51], v[96:99], v[160:175]\n"
    "ds_read_b128 v[216:219], v242 offset:29696\n"
    "v_exp_f32_e32 v114, v114\n"
    "v_exp_f32_e32 v115, v115\n"
    "v_exp_f32_e64 v116, v116\n"
    "v_mfma_f32_32x32x16_bf16 v[160:175], v[52:55], v[104:107], v[160:175]\n"
    "ds_read_b128 v[220:223], v244 offset:8192\n"
    "v_exp_f32_e32 v117, v117\n"
    "v_exp_f32_e32 v118, v118\n"
    "v_exp_f32_e64 v119, v119\n"
    "v_mfma_f32_16x16x32_bf16 v[188:191], v[236:239], v[104:107], v[188:191]\n"
    "ds_read_b128 v[224:227], v242 offset:30720\n"
    "v_cvt_pk_bf16_f32 v112, v112, v113\n"
    "v_mfma_f32_32x32x16_bf16 v[96:111], v[24:27], v[204:207], 0\n"
    "s_waitcnt lgkmcnt(4)\n"
    "v_cvt_pk_bf16_f32 v113, v114, v115\n"
    "v_cvt_pk_bf16_f32 v114, v116, v117\n"
    "v_cvt_pk_bf16_f32 v115, v118, v119\n"
    "v_exp_f32_e32 v120, v120\n"
    "v_exp_f32_e64 v121, v121\n"
    "v_mfma_f32_32x32x16_bf16 v[96:111], v[28:31], v[208:211], v[96:111]\n"
    "s_waitcnt lgkmcnt(3)\n"
    "v_exp_f32_e32 v122, v122\n"
    "v_exp_f32_e32 v123, v123\n"
    "v_exp_f32_e32 v124, v124\n"
    "v_mfma_f32_32x32x16_bf16 v[160:175], v[56:59], v[112:115], v[160:175]\n"
    "s_waitcnt lgkmcnt(2)\n"
    "v_exp_f32_e32 v125, v125\n"
    "v_exp_f32_e32 v126, v126\n"
    "v_exp_f32_e32 v127, v127\n"
    "v_mfma_f32_16x16x32_bf16 v[188:191], v[236:239], v[112:115], v[188:191]\n"
    "s_waitcnt lgkmcnt(1)\n"
    "v_cvt_pk_bf16_f32 v120, v120, v121\n"
    "v_cvt_pk_bf16_f32 v121, v122, v123\n"
    "v_cvt_pk_bf16_f32 v122, v124, v125\n"
    "v_cvt_pk_bf16_f32 v123, v126, v127\n"
    "v_exp_f32_e32 v128, v128\n"
    "v_exp_f32_e64 v129, v129\n"
    "v_mfma_f32_32x32x16_bf16 v[160:175], v[60:63], v[120:123], v[160:175]\n"
    "s_waitcnt lgkmcnt(0)\n"
    "v_exp_f32_e32 v130, v130\n"
    "v_exp_f32_e32 v131, v131\n"
    "v_exp_f32_e32 v132, v132\n"
    "v_mfma_f32_16x16x32_bf16 v[188:191], v[236:239], v[120:123], v[188:191]\n"
    "v_exp_f32_e64 v133, v133\n"
    "v_mfma_f32_32x32x16_bf16 v[112:127], v[32:35], v[204:207], 0\n"
    "v_exp_f32_e32 v134, v134\n"
    "v_exp_f32_e32 v135, v135\n"
    "v_cvt_pk_bf16_f32 v128, v128, v129\n"
    "v_cvt_pk_bf16_f32 v129, v130, v131\n"
    "v_mfma_f32_32x32x16_bf16 v[112:127], v[36:39], v[208:211], v[112:127]\n"
    "v_cvt_pk_bf16_f32 v130, v132, v133\n"
    "v_cvt_pk_bf16_f32 v131, v134, v135\n"
    "v_exp_f32_e32 v136, v136\n"
    "v_exp_f32_e32 v137, v137\n"
    "v_mfma_f32_32x32x16_bf16 v[160:175], v[64:67], v[128:131], v[160:175]\n"
    "v_exp_f32_e32 v138, v138\n"
    "v_exp_f32_e32 v139, v139\n"
    "v_exp_f32_e64 v140, v140\n"
    "v_mfma_f32_16x16x32_bf16 v[188:191], v[236:239], v[128:131], v[188:191]\n"
    "v_exp_f32_e32 v141, v141\n"
    "v_exp_f32_e32 v142, v142\n"
    "v_exp_f32_e64 v143, v143\n"
    "v_cvt_pk_bf16_f32 v228, v136, v137\n"
    "v_cvt_pk_bf16_f32 v229, v138, v139\n"
    "v_cvt_pk_bf16_f32 v230, v140, v141\n"
    "v_cvt_pk_bf16_f32 v231, v142, v143\n"
    "v_mfma_f32_32x32x16_bf16 v[128:143], v[40:43], v[204:207], 0\n"
    "v_max3_f32 v200, v96, v97, v98\n"
    "v_max3_f32 v200, v200, v99, v99\n"
    "v_max3_f32 v200, v200, v100, v101\n"
    "v_max3_f32 v200, v200, v102, v103\n"
    "v_max3_f32 v200, v200, v104, v105\n"
    "v_max3_f32 v200, v200, v106, v107\n"
    "v_mfma_f32_32x32x16_bf16 v[128:143], v[44:47], v[208:211], v[128:143]\n"
    "v_max3_f32 v200, v200, v108, v109\n"
    "v_max3_f32 v200, v200, v110, v111\n"
    "v_max3_f32 v200, v200, v112, v113\n"
    "v_max3_f32 v200, v200, v114, v115\n"
    "v_max3_f32 v200, v200, v116, v117\n"
    "v_max3_f32 v200, v200, v118, v119\n"
    "v_mfma_f32_32x32x16_bf16 v[160:175], v[68:71], v[228:231], v[160:175]\n"
    "v_max3_f32 v200, v200, v120, v121\n"
    "v_max3_f32 v200, v200, v122, v123\n"
    "v_max3_f32 v200, v200, v124, v125\n"
    "v_max3_f32 v200, v200, v126, v127\n"
    "v_max3_f32 v200, v200, v184, v185\n"
    "v_max3_f32 v200, v200, v128, v129\n"
    "v_mfma_f32_16x16x32_bf16 v[188:191], v[236:239], v[228:231], v[188:191]\n"
    "ds_read_b128 v[228:231], v245 offset:8192\n"
    "v_max3_f32 v200, v200, v130, v131\n"
    "v_max3_f32 v200, v200, v132, v133\n"
    "v_max3_f32 v200, v200, v134, v135\n"
    "v_max3_f32 v200, v200, v136, v137\n"
    "v_max3_f32 v200, v200, v138, v139\n"
    "v_max3_f32 v200, v200, v140, v141\n"
    "v_max3_f32 v200, v200, v142, v143\n"
    "v_mov_b32_e32 v201, v200\n"
    "v_rcp_f32_e32 v203, v188\n"
    "s_nop 0\n"
    "v_permlane32_swap_b32_e32 v200, v201\n"
    "v_max_f32_e32 v200, v200, v201\n"
    "v_and_b32_e32 v201, s59, v200\n"
    "v_sub_f32_e64 v201, v200, v201\n"
    "v_perm_b32 v198, v201, v200, s58\n"
    "v_and_b32_e32 v199, s59, v201\n"
    "v_sub_f32_e32 v199, v201, v199\n"
    "v_lshrrev_b32_e32 v199, 16, v199\n"
    "v_sub_f32_e32 v184, v184, v200\n"
    "v_sub_f32_e64 v185, v185, v200\n"
    "v_mfma_f32_32x32x8_bf16 v[96:111], v[240:241], v[198:199], v[96:111]\n"
    "v_exp_f32_e32 v184, v184\n"
    "v_exp_f32_e32 v185, v185\n"
    "v_fmac_f32_e32 v144, v160, v203\n"
    "v_fmac_f32_e32 v145, v161, v203\n"
    "v_fmac_f32_e64 v146, v162, v203\n"
    "v_mfma_f32_32x32x8_bf16 v[112:127], v[240:241], v[198:199], v[112:127]\n"
    "v_fmac_f32_e32 v147, v163, v203\n"
    "v_fmac_f32_e32 v148, v164, v203\n"
    "v_fmac_f32_e32 v149, v165, v203\n"
    "v_fmac_f32_e32 v150, v166, v203\n"
    "v_fmac_f32_e32 v151, v167, v203\n"
    "v_fmac_f32_e32 v152, v168, v203\n"
    "v_fmac_f32_e64 v153, v169, v203\n"
    "v_mfma_f32_32x32x8_bf16 v[128:143], v[240:241], v[198:199], v[128:143]\n"
    "v_fmac_f32_e32 v154, v170, v203\n"
    "v_fmac_f32_e32 v155, v171, v203\n"
    "v_fmac_f32_e32 v156, v172, v203\n"
    "v_fmac_f32_e32 v157, v173, v203\n"
    "v_fmac_f32_e32 v158, v174, v203\n"
    "v_fmac_f32_e32 v159, v175, v203\n"
    "v_cvt_pk_bf16_f32 v144, v144, v145\n"
    "v_mfma_f32_32x32x16_bf16 v[160:175], v[232:235], v[212:215], 0\n"
    "ds_read_b128 v[232:235], v242 offset:31744\n"
    "v_cvt_pk_bf16_f32 v145, v146, v147\n"
    "v_cvt_pk_bf16_f32 v146, v148, v149\n"
    "v_cvt_pk_bf16_f32 v147, v150, v151\n"
    "v_pk_max_i16 v144, v144, 0\n"
    "v_pk_max_i16 v145, v145, 0\n"
    "v_mfma_f32_32x32x16_bf16 v[160:175], v[216:219], v[220:223], v[160:175]\n"
    "ds_read_b128 v[212:215], v246 offset:8192\n"
    "v_pk_max_i16 v146, v146, 0\n"
    "v_pk_max_i16 v147, v147, 0\n"
    "v_cvt_pk_bf16_f32 v152, v152, v153\n"
    "v_cvt_pk_bf16_f32 v153, v154, v155\n"
    "v_cvt_pk_bf16_f32 v154, v156, v157\n"
    "s_waitcnt lgkmcnt(2)\n"
    "v_mfma_f32_32x32x16_bf16 v[160:175], v[224:227], v[228:231], v[160:175]\n"
    "global_store_dwordx4 v251, v[144:147], s[46:47] offset:-4096\n"
    "v_cvt_pk_bf16_f32 v155, v158, v159\n"
    "v_pk_max_i16 v152, v152, 0\n"
    "v_pk_max_i16 v153, v153, 0\n"
    "v_pk_max_i16 v154, v154, 0\n"
    "v_pk_max_i16 v155, v155, 0\n"
    "v_cvt_pk_bf16_f32 v196, v184, v185\n"
    "v_exp_f32_e32 v96, v96\n"
    "v_exp_f32_e64 v97, v97\n"
    "v_mfma_f32_16x16x16_bf16 v[188:191], v[236:237], v[196:197], 0\n"
    "v_exp_f32_e32 v98, v98\n"
    "v_exp_f32_e32 v99, v99\n"
    "v_exp_f32_e32 v100, v100\n"
    "v_exp_f32_e32 v101, v101\n"
    "v_exp_f32_e32 v102, v102\n"
    "v_exp_f32_e32 v103, v103\n"
    "v_cvt_pk_bf16_f32 v96, v96, v97\n"
    "v_cvt_pk_bf16_f32 v97, v98, v99\n"
    "s_waitcnt lgkmcnt(0)\n"
    "v_mfma_f32_32x32x16_bf16 v[160:175], v[232:235], v[212:215], v[160:175]\n"
    "global_store_dwordx4 v251, v[152:155], s[46:47] offset:-4064\n"
    "v_cvt_pk_bf16_f32 v98, v100, v101\n"
    "v_cvt_pk_bf16_f32 v99, v102, v103\n"
    "v_exp_f32_e32 v104, v104\n"
    "v_exp_f32_e64 v105, v105\n"
    "v_mfma_f32_32x32x8_bf16 v[144:159], v[194:195], v[196:197], 0\n"
    "v_exp_f32_e32 v106, v106\n"
    "v_exp_f32_e32 v107, v107\n"
    "v_exp_f32_e32 v108, v108\n"
    "v_exp_f32_e32 v109, v109\n"
    "v_mfma_f32_16x16x32_bf16 v[188:191], v[236:239], v[96:99], v[188:191]\n"
    "s_cmp_lt_i32 s37, s36\n"
    "s_cbranch_scc0 .Lskip3_%=\n"
    "s_add_u32 s50, s40, 8192\n"
    "s_addc_u32 s51, s41, 0\n"
    "s_add_u32 m0, s52, 8192\n"
    "s_nop 0\n"
    "global_load_lds_dwordx4 v252, s[50:51]\n"
    "global_load_lds_dwordx4 v253, s[50:51] offset:1024\n"
    "global_load_lds_dwordx4 v252, s[50:51] offset:2048\n"
    "global_load_lds_dwordx4 v253, s[50:51] offset:3072\n"
    ".Lskip3_%=:\n"
    "v_mfma_f32_32x32x16_bf16 v[144:159], v[72:75], v[96:99], v[144:159]\n"
    "v_exp_f32_e32 v110, v110\n"
    "v_exp_f32_e32 v111, v111\n"
    "v_cvt_pk_bf16_f32 v104, v104, v105\n"
    "v_cvt_pk_bf16_f32 v105, v106, v107\n"
    "v_cvt_pk_bf16_f32 v106, v108, v109\n"
    "v_cvt_pk_bf16_f32 v107, v110, v111\n"
    "v_exp_f32_e32 v112, v112\n"
    "v_exp_f32_e32 v113, v113\n"
    "v_mfma_f32_32x32x16_bf16 v[144:159], v[76:79], v[104:107], v[144:159]\n"
    "v_exp_f32_e32 v114, v114\n"
    "v_exp_f32_e32 v115, v115\n"
    "v_exp_f32_e64 v116, v116\n"
    "v_mfma_f32_16x16x32_bf16 v[188:191], v[236:239], v[104:107], v[188:191]\n"
    "v_exp_f32_e32 v117, v117\n"
    "v_exp_f32_e32 v118, v118\n"
    "v_exp_f32_e64 v119, v119\n"
    "v_cvt_pk_bf16_f32 v112, v112, v113\n"
    "v_cvt_pk_bf16_f32 v113, v114, v115\n"
    "v_cvt_pk_bf16_f32 v114, v116, v117\n"
    "v_cvt_pk_bf16_f32 v115, v118, v119\n"
    "v_exp_f32_e32 v120, v120\n"
    "v_exp_f32_e32 v121, v121\n"
    "v_mfma_f32_32x32x16_bf16 v[144:159], v[80:83], v[112:115], v[144:159]\n"
    "v_exp_f32_e32 v122, v122\n"
    "v_exp_f32_e32 v123, v123\n"
    "v_exp_f32_e64 v124, v124\n"
    "v_mfma_f32_16x16x32_bf16 v[188:191], v[236:239], v[112:115], v[188:191]\n"
    "v_exp_f32_e32 v125, v125\n"
    "v_exp_f32_e32 v126, v126\n"
    "v_exp_f32_e64 v127, v127\n"
    "v_cvt_pk_bf16_f32 v120, v120, v121\n"
    "v_cvt_pk_bf16_f32 v121, v122, v123\n"
    "v_cvt_pk_bf16_f32 v122, v124, v125\n"
    "v_cvt_pk_bf16_f32 v123, v126, v127\n"
    "v_exp_f32_e32 v128, v128\n"
    "v_exp_f32_e32 v129, v129\n"
    "v_mfma_f32_32x32x16_bf16 v[144:159], v[84:87], v[120:123], v[144:159]\n"
    "v_exp_f32_e32 v130, v130\n"
    "v_exp_f32_e32 v131, v131\n"
    "v_exp_f32_e64 v132, v132\n"
    "v_mfma_f32_16x16x32_bf16 v[188:191], v[236:239], v[120:123], v[188:191]\n"
    "v_exp_f32_e32 v133, v133\n"
    "v_exp_f32_e32 v134, v134\n"
    "v_exp_f32_e64 v135, v135\n"
    "v_cvt_pk_bf16_f32 v128, v128, v129\n"
    "v_cvt_pk_bf16_f32 v129, v130, v131\n"
    "v_cvt_pk_bf16_f32 v130, v132, v133\n"
    "v_cvt_pk_bf16_f32 v131, v134, v135\n"
    "v_exp_f32_e32 v136, v136\n"
    "v_exp_f32_e32 v137, v137\n"
    "v_mfma_f32_32x32x16_bf16 v[144:159], v[88:91], v[128:131], v[144:159]\n"
    "v_exp_f32_e32 v138, v138\n"
    "v_exp_f32_e32 v139, v139\n"
    "v_exp_f32_e64 v140, v140\n"
    "v_mfma_f32_16x16x32_bf16 v[188:191], v[236:239], v[128:131], v[188:191]\n"
    "v_exp_f32_e32 v141, v141\n"
    "v_exp_f32_e32 v142, v142\n"
    "v_exp_f32_e64 v143, v143\n"
    "v_cvt_pk_bf16_f32 v136, v136, v137\n"
    "v_cvt_pk_bf16_f32 v137, v138, v139\n"
    "v_cvt_pk_bf16_f32 v138, v140, v141\n"
    "v_cvt_pk_bf16_f32 v139, v142, v143\n"
    "s_nop 1\n"
    "v_mfma_f32_32x32x16_bf16 v[144:159], v[92:95], v[136:139], v[144:159]\n"
    "v_mfma_f32_16x16x32_bf16 v[188:191], v[236:239], v[136:139], v[188:191]\n"
    "s_nop 7\n"
    "v_rcp_f32_e32 v203, v188\n"
    "s_nop 0\n"
    "v_fmac_f32_e32 v160, v144, v203\n"
    "v_fmac_f32_e32 v161, v145, v203\n"
    "v_fmac_f32_e32 v162, v146, v203\n"
    "v_fmac_f32_e32 v163, v147, v203\n"
    "v_fmac_f32_e32 v164, v148, v203\n"
    "v_fmac_f32_e32 v165, v149, v203\n"
    "v_fmac_f32_e32 v166, v150, v203\n"
    "v_fmac_f32_e32 v167, v151, v203\n"
    "v_fmac_f32_e32 v168, v152, v203\n"
    "v_fmac_f32_e32 v169, v153, v203\n"
    "v_fmac_f32_e32 v170, v154, v203\n"
    "v_fmac_f32_e32 v171, v155, v203\n"
    "v_fmac_f32_e32 v172, v156, v203\n"
    "v_fmac_f32_e32 v173, v157, v203\n"
    "v_fmac_f32_e32 v174, v158, v203\n"
    "v_fmac_f32_e32 v175, v159, v203\n"
    "v_cvt_pk_bf16_f32 v160, v160, v161\n"
    "v_cvt_pk_bf16_f32 v161, v162, v163\n"
    "v_cvt_pk_bf16_f32 v162, v164, v165\n"
    "v_cvt_pk_bf16_f32 v163, v166, v167\n"
    "v_pk_max_i16 v160, v160, 0\n"
    "v_pk_max_i16 v161, v161, 0\n"
    "v_pk_max_i16 v162, v162, 0\n"
    "v_pk_max_i16 v163, v163, 0\n"
    "v_cvt_pk_bf16_f32 v168, v168, v169\n"
    "v_cvt_pk_bf16_f32 v169, v170, v171\n"
    "v_cvt_pk_bf16_f32 v170, v172, v173\n"
    "v_cvt_pk_bf16_f32 v171, v174, v175\n"
    "v_pk_max_i16 v168, v168, 0\n"
    "v_pk_max_i16 v169, v169, 0\n"
    "v_pk_max_i16 v170, v170, 0\n"
    "v_pk_max_i16 v171, v171, 0\n"
    "global_store_dwordx4 v251, v[160:163], s[46:47] offset:-4032\n"
    "global_store_dwordx4 v251, v[168:171], s[46:47] offset:-4000\n"
    "s_add_u32 s42, s42, s39\n"
    "s_addc_u32 s43, s43, 0\n"
    "s_add_u32 s40, s40, s39\n"
    "s_addc_u32 s41, s41, 0\n"
    "s_cmp_lt_i32 s37, s36\n"
    "s_cbranch_scc1 .Lloop_%=\n"
      : "+{s37}"(bb), "+{s[40:41]}"(nsrc), "+{s[42:43]}"(dst)
      : "{s36}"(bend), "{s38}"(nw), "{s39}"(step), "{s52}"(ebase),
        "{v236}"(one2), "{v237}"(one2), "{v238}"(one2), "{v239}"(one2), "{v240}"(neg0), "{v241}"(neg1), "{v242}"(wadr),
        "{v243}"(ea0), "{v244}"(ea1), "{v245}"(ea2), "{v246}"(ea3), "{v247}"(lh0), "{v248}"(lh1), "{v249}"(lk), "{v250}"(lw),
        "{v251}"(ooff), "{v252}"(voff_e), "{v253}"(voff_o)
      : "v0", "v1", "v2", "v3", "v4", "v5", "v6", "v7", "v8", "v9", "v10", "v11", "v12", "v13", "v14", "v15", "v16", "v17", "v18", "v19", "v20", "v21", "v22", "v23", "v24", "v25", "v26", "v27", "v28", "v29", "v30", "v31", "v32", "v33", "v34", "v35", "v36", "v37", "v38", "v39", "v40", "v41", "v42", "v43", "v44", "v45", "v46", "v47", "v48", "v49", "v50", "v51", "v52", "v53", "v54", "v55", "v56", "v57", "v58", "v59", "v60", "v61", "v62", "v63", "v64", "v65", "v66", "v67", "v68", "v69", "v70", "v71", "v72", "v73", "v74", "v75", "v76", "v77", "v78", "v79", "v80", "v81", "v82", "v83", "v84", "v85", "v86", "v87", "v88", "v89", "v90", "v91", "v92", "v93", "v94", "v95", "v96", "v97", "v98", "v99", "v100", "v101", "v102", "v103", "v104", "v105", "v106", "v107", "v108", "v109", "v110", "v111", "v112", "v113", "v114", "v115", "v116", "v117", "v118", "v119", "v120", "v121", "v122", "v123", "v124", "v125", "v126", "v127", "v128", "v129", "v130", "v131", "v132", "v133", "v134", "v135", "v136", "v137", "v138", "v139", "v140", "v141", "v142", "v143", "v144", "v145", "v146", "v147", "v148", "v149", "v150", "v151", "v152", "v153", "v154", "v155", "v156", "v157", "v158", "v159", "v160", "v161", "v162", "v163", "v164", "v165", "v166", "v167", "v168", "v169", "v170", "v171", "v172", "v173", "v174", "v175", "v176", "v177", "v178", "v179", "v180", "v181", "v182", "v183", "v184", "v185", "v186", "v187", "v188", "v189", "v190", "v191", "v192", "v193", "v194", "v195", "v196", "v197", "v198", "v199", "v200", "v201", "v202", "v203", "v204", "v205", "v206", "v207", "v208", "v209", "v210", "v211", "v212", "v213", "v214", "v215", "v216", "v217", "v218", "v219", "v220", "v221", "v222", "v223", "v224", "v225", "v226", "v227", "v228", "v229", "v230", "v231", "v232", "v233", "v234", "v235", "s44", "s45", "s46", "s47", "s48", "s49", "s50", "s51", "s53", "s54", "s55", "s56", "s57", "s58", "s59", "s60", "s61", "s62", "s63", "scc", "vcc", "memory");
  }
}

extern "C" void kernel_launch(void* const* d_in, const int* in_sizes, int n_in, void* d_out, int out_size, void* d_ws, size_t ws_size,
                              hipStream_t stream) {
  const int B = in_sizes[0] / (F * D);
  int grid = (B + NWAVE - 1) / NWAVE;
  if (grid > 256) grid = 256;
  if (grid < 1) return;
  if ((B + grid - 1) / grid > 16) grid = (B + 15) / 16;
  autoint_kernel<<<grid, 256, 0, stream>>>((const char*)d_in[0], (const char*)d_in[1], (const char*)d_in[2], (const char*)d_in[3],
                                           (const char*)d_in[4], (char*)d_out, B, grid * NWAVE, B - 1, 0);
}
```
